```python
import torch

def _softmax(x, axis=-1):
    return torch.softmax(x, dim=axis)
def _gelu(x):
    return torch.nn.functional.gelu(x, approximate="tanh")
def _silu(x):
    return torch.nn.functional.silu(x)
def _sigmoid(x):
    return torch.sigmoid(x)
def _layernorm(x, axis=-1, eps=1e-5):
    m = x.mean(dim=axis, keepdim=True)
    v = x.var(dim=axis, keepdim=True, unbiased=False)
    return (x - m) / torch.sqrt(v + eps)
def _scatter_add(tgt, idx, src, axis=0):
    return tgt.clone().index_add_(axis, idx, src)
def _segment_sum(src, seg, n):
    out = torch.zeros((n,) + src.shape[1:], dtype=src.dtype)
    return out.index_add_(0, seg, src)
def _pad(x, pw):
    flat = tuple(v for pair in reversed(pw) for v in pair)
    return torch.nn.functional.pad(x, flat)

kernel_name = 'parallel_sum__ssm_conv_h3__glu_gated_swiglu__prompt'

def setup_inputs():

    _w = (lambda a: (lambda t: t.to(torch.bfloat16) if t.is_floating_point() else t)(torch.as_tensor(a)))
    def _A_setup():
        B, S, D = 1, 2048, 1024
        ks = 4
        return {
            "q": _w((lambda *s: torch.randn(*s, dtype=torch.bfloat16) * 0.05)(B, S, D)),
            "k": _w((lambda *s: torch.randn(*s, dtype=torch.bfloat16) * 0.2)(B, S, D)),
            "v": _w((lambda *s: torch.randn(*s, dtype=torch.bfloat16) * 0.2)(B, S, D)),
            "shift_k": _w((lambda *s: torch.randn(*s, dtype=torch.bfloat16) * 0.2)(D, ks) * 0.5),
            "ssm_k": _w((lambda *s: torch.randn(*s, dtype=torch.bfloat16) * 0.4)(D, S) * (1.0 / S ** 0.5)),
            "ssm_D": _w((lambda *s: torch.randn(*s, dtype=torch.bfloat16) * 0.2)(D)),
        }
    _A_in = _A_setup()

    def _B_setup():
        B, S, D, F = 1, 2048, 1024, 3584
        return {
            "x": _w((lambda *s: torch.randn(*s, dtype=torch.bfloat16) * 0.05)(B, S, D)),
            "w1": _w((lambda *s: torch.randn(*s, dtype=torch.bfloat16) * 0.2)(D, F) * (2.0 / D) ** 0.5),
            "w3": _w((lambda *s: torch.randn(*s, dtype=torch.bfloat16) * 0.2)(D, F) * (2.0 / D) ** 0.5),
            "w2": _w((lambda *s: torch.randn(*s, dtype=torch.bfloat16) * 0.2)(F, D) * (2.0 / F) ** 0.5),
        }
    _B_in = _B_setup()

    return {
        "A_q": _A_in["q"],
        "A_k": _A_in["k"],
        "A_v": _A_in["v"],
        "A_shift_k": _A_in["shift_k"],
        "A_ssm_k": _A_in["ssm_k"],
        "A_ssm_D": _A_in["ssm_D"],
        "B_x": _B_in["x"],
        "B_w1": _B_in["w1"],
        "B_w3": _B_in["w3"],
        "B_w2": _B_in["w2"],
    }

def reference(A_q, A_k, A_v, A_shift_k, A_ssm_k, A_ssm_D, B_x, B_w1, B_w3, B_w2):
    def _A_ref(q, k, v, shift_k, ssm_k, ssm_D):
        B, S, D = q.shape
        ks = shift_k.shape[-1]
        k_pad = _pad(k, ((0, 0), (ks - 1, 0), (0, 0)))
        k_shift = torch.zeros_like(k)
        for t in range(ks):
            k_shift = k_shift + k_pad[:, t:t + S, :] * shift_k[:, ks - 1 - t][None, None, :]
        kv = k_shift * v
        L = 2 * S
        kv_pad = _pad(kv, ((0, 0), (0, S), (0, 0)))
        kf_pad = _pad(ssm_k, ((0, 0), (0, S)))
        kv_f = (lambda x, axes=(-2,-1): torch.fft.rfft2(x.float(), dim=axes))(kv_pad[:, None, :, :], axes=(1, 2))
        kf_f = (lambda x, axes=(-2,-1): torch.fft.rfft2(x.float(), dim=axes))(kf_pad[:, None, :], axes=(1, 2))
        kf_f = (lambda x, *d: x.permute(*d))(kf_f, 1, 2, 0)
        y_f = kv_f * kf_f[None, :, :, :]
        y = (lambda x, s, axes=(-2,-1): torch.fft.irfft2(x, s=s, dim=axes).to(torch.bfloat16))(y_f, (1, L), axes=(1, 2))[:, 0, :S, :]
        y = y + kv * ssm_D[None, None, :]
        return q * y

    def _B_ref(x, w1, w3, w2):
        gate = torch.einsum("bsd,df->bsf", x, w1)
        up = torch.einsum("bsd,df->bsf", x, w3)
        h = _silu(gate) * up
        out = torch.einsum("bsf,fd->bsd", h, w2)
        return out

    x = A_q
    xn = _layernorm(x, axis=-1)
    return x + _A_ref(xn, A_k, A_v, A_shift_k, A_ssm_k, A_ssm_D) + _B_ref(xn, B_w1, B_w3, B_w2)
```

```cpp
#include <hip/hip_runtime.h>
#include <hip/hip_bf16.h>

#define LAS __attribute__((address_space(3)))
#define GAS __attribute__((address_space(1)))
typedef unsigned short u16;
typedef short s16x4 __attribute__((ext_vector_type(4)));
typedef short s16x8 __attribute__((ext_vector_type(8)));
typedef __bf16 bf16x2 __attribute__((ext_vector_type(2)));
typedef __bf16 bf16x8 __attribute__((ext_vector_type(8)));
typedef float f32x2 __attribute__((ext_vector_type(2)));
typedef float f32x4 __attribute__((ext_vector_type(4)));
typedef unsigned u32x4 __attribute__((ext_vector_type(4)));
typedef unsigned u32x2 __attribute__((ext_vector_type(2)));

constexpr int S_ = 2048, D_ = 1024, F_ = 3584;

constexpr unsigned WS_EP = 0;
constexpr unsigned WS_FU = 4096;
constexpr unsigned WS_FH = WS_FU + 32768;
constexpr unsigned WS_FZ = WS_FH + 32768;
constexpr unsigned WS_ST = WS_FZ + 32768;
constexpr unsigned WS_U = 1u << 20;
constexpr unsigned WS_Z = WS_U + S_ * D_ * 2;
constexpr unsigned WS_EX = WS_Z + S_ * D_ * 2;
constexpr unsigned WS_H = WS_EX + 256 * 16384;
constexpr unsigned WS_END = WS_H + S_ * F_ * 2;
constexpr unsigned SPIN_CAP = 1u << 18;

__device__ __forceinline__ float bflo(unsigned v) { return __uint_as_float(v << 16); }
__device__ __forceinline__ float bfhi(unsigned v) { return __uint_as_float(v & 0xffff0000u); }
__device__ __forceinline__ unsigned pk_bf16(float lo, float hi) {
  f32x2 v = {lo, hi};
  return __builtin_bit_cast(unsigned, __builtin_convertvector(v, bf16x2));
}
__device__ __forceinline__ float rbf(float x) { return bflo(pk_bf16(x, x)); }
__device__ __forceinline__ float dot2(unsigned a, unsigned b, float c) {
  return __builtin_amdgcn_fdot2_f32_bf16(__builtin_bit_cast(bf16x2, a), __builtin_bit_cast(bf16x2, b), c, false);
}
template <int CTRL> __device__ __forceinline__ float dpp(float v) {
  return __int_as_float(__builtin_amdgcn_update_dpp(0, __float_as_int(v), CTRL, 0xf, 0xf, true));
}
__device__ __forceinline__ float add32(float a, float b) {
  auto r = __builtin_amdgcn_permlane32_swap(__float_as_uint(a), __float_as_uint(b), false, false);
  return __uint_as_float(r[0]) + __uint_as_float(r[1]);
}
__device__ __forceinline__ float add16(float a, float b) {
  auto r = __builtin_amdgcn_permlane16_swap(__float_as_uint(a), __float_as_uint(b), false, false);
  return __uint_as_float(r[0]) + __uint_as_float(r[1]);
}
__device__ __forceinline__ float sum64(float v) {
  v += dpp<0xB1>(v); v += dpp<0x4E>(v); v += dpp<0x141>(v); v += dpp<0x140>(v);
  v = add16(v, v);
  return add32(v, v);
}
__device__ __forceinline__ u32x4 swap16(u32x2 a, u32x2 b) {
  auto r0 = __builtin_amdgcn_permlane16_swap(a[0], b[0], false, false);
  auto r1 = __builtin_amdgcn_permlane16_swap(a[1], b[1], false, false);
  return (u32x4){r0[0], r1[0], r0[1], r1[1]};
}
__device__ __forceinline__ f32x4 mfma16(s16x8 a, s16x8 b, f32x4 c) {
  return __builtin_amdgcn_mfma_f32_16x16x32_bf16(__builtin_bit_cast(bf16x8, a), __builtin_bit_cast(bf16x8, b), c, 0, 0, 0);
}
__device__ __forceinline__ void mfma_acc(f32x4& c, s16x8 a, s16x8 b) { asm volatile("v_mfma_f32_16x16x32_bf16 %0, %1, %2, %0" : "+v"(c) : "v"(a), "v"(b)); }
#define MFMA_DONE() do { asm volatile("s_nop 7\n\ts_nop 7"); __builtin_amdgcn_sched_barrier(0); } while (0)
__device__ __forceinline__ s16x4 tr_read(LAS char* p) { return __builtin_amdgcn_ds_read_tr16_b64_v4i16((LAS s16x4*)p); }
__device__ __forceinline__ s16x8 cat8(s16x4 a, s16x4 b) { return __builtin_shufflevector(a, b, 0, 1, 2, 3, 4, 5, 6, 7); }
template <int OFF, int DOFF = 0> __device__ __forceinline__ void dma16(const void* base, unsigned voff, unsigned dst) {
  asm volatile("s_add_u32 m0, %0, %4\n\ts_nop 0\n\tglobal_load_lds_dwordx4 %1, %2 offset:%3" ::"s"(dst), "v"(voff), "s"(base), "i"(OFF), "i"(DOFF - OFF) : "scc");
}
template <int DOFF> __device__ __forceinline__ void dma16_m(const void* base, unsigned voff, unsigned dst, unsigned long long mask) {
  asm volatile("s_add_u32 m0, %0, %4\n\ts_mov_b64 exec, %3\n\tglobal_load_lds_dwordx4 %1, %2\n\ts_mov_b64 exec, -1"
               ::"s"(dst), "v"(voff), "s"(base), "s"(mask), "i"(DOFF) : "scc");
}
template <int V, int N, int SH> __device__ __forceinline__ void touch_if(int sel, const void* base, unsigned dst) {
  unsigned t;
  asm volatile("s_cmp_lg_u32 %1, %4\n\ts_cbranch_scc1 1f\n\tv_mbcnt_lo_u32_b32 %0, -1, 0\n\tv_mbcnt_hi_u32_b32 %0, -1, %0\n\t"
               "v_min_u32 %0, %5, %0\n\tv_lshlrev_b32 %0, %6, %0\n\ts_mov_b32 m0, %3\n\ts_nop 0\n\tglobal_load_lds_dword %0, %2\n1:"
               : "=&v"(t) : "s"(sel), "s"(base), "s"(dst), "i"(V), "i"(N - 1), "i"(SH) : "scc");
}
__device__ __forceinline__ void flag_if(int c, const void* p, unsigned val) {
  unsigned a, b;
  asm volatile("s_cmp_lg_u32 %2, 0\n\ts_cbranch_scc1 1f\n\tv_mov_b32 %0, 0\n\tv_mov_b32 %1, %4\n\ts_mov_b64 exec, 1\n\t"
               "global_store_dword %0, %1, %3 sc1\n\ts_mov_b64 exec, -1\n1:" : "=&v"(a), "=&v"(b) : "s"(c), "s"(p), "s"(val) : "scc", "memory");
}
__device__ __forceinline__ void colsum(float& cs, s16x8 w, int idx) {
  const u32x4 v = __builtin_bit_cast(u32x4, w);
  unsigned a, b;
  asm volatile("s_set_gpr_idx_on %3, gpr_idx(SRC1)\n\tv_lshlrev_b32 %1, 16, %4 ; CSCHK %4 %5\n\tv_and_b32 %2, 0xffff0000, %4\n\ts_set_gpr_idx_off\n\t"
               "v_add_f32 %0, %0, %1\n\tv_add_f32 %0, %0, %2" : "+v"(cs), "=&v"(a), "=&v"(b) : "s"(idx), "v"(v[0]), "v"(v));
}
#define OPAQUE(x) asm volatile("" : "+v"(x))
#define WAIT_V(n) asm volatile("s_waitcnt vmcnt(%0)" ::"i"(n) : "memory")
#define WAIT_L0() __builtin_amdgcn_s_waitcnt(0xC07F)
#define WAIT_V0C() __builtin_amdgcn_s_waitcnt(0x0F70)
#define WAIT_VL(n) do { WAIT_L0(); WAIT_V(n); } while (0)
#define FENCE_C() asm volatile("" ::: "memory")
#define BAR() do { FENCE_C(); __builtin_amdgcn_s_barrier(); FENCE_C(); } while (0)
#define SCHED0() __builtin_amdgcn_sched_barrier(0)
#define RLX __ATOMIC_RELAXED, __HIP_MEMORY_SCOPE_AGENT
#define ST_SC1(v, off) __builtin_amdgcn_raw_buffer_store_b128(v, rs, off, 0, 16)
#define LD_SC1(off) __builtin_amdgcn_raw_buffer_load_b128(rs, off, 0, 16)

#define STAMP(i)
#define STAMP_OUT()
#define STEP_T(idx)
template <int V> struct IC { static constexpr int value = V; };
template <int N, int I = 0, class Fn> __device__ __forceinline__ void static_for(Fn&& f) {
  if constexpr (I < N) { f(IC<I>{}); static_for<N, I + 1>(f); }
}

__device__ __forceinline__ void wait_flags(GAS unsigned* p, unsigned v, unsigned tag) {
  for (unsigned spins = 0; !__all(v == tag) && spins < SPIN_CAP; ++spins) {
    __builtin_amdgcn_s_sleep(2);
    v = __hip_atomic_load(p, RLX);
  }
  FENCE_C();
}

constexpr int U_GS = 960;
constexpr int U_UP = 8 * U_GS + 16;
constexpr int U_WS = U_UP + 8 * U_GS;
constexpr int L_A = 61568;
constexpr int L_ST = L_A + 3 * 32768;
constexpr int L_JUNK = L_ST + 2048;
constexpr int L_SK = L_JUNK + 256;
constexpr int L_CNT = L_SK + 512;
constexpr int L_END = L_CNT + 16;
constexpr int L_ALLOC = L_END;
constexpr int abuf(int u) { return L_A + (u % 3 == 0 ? 0 : u % 3 == 1 ? 65536 : 32768); }
constexpr int PSTEP = 13;
constexpr int L_CSP = 0, L_CSF = 4096;
constexpr int C_US = 256 + 4096, L_U = 98304;
constexpr int L_Z = L_U + 4 * C_US;
constexpr int C_TS = 32 + 4096, L_T = 143232;
constexpr int CSPLIT = 22;
static_assert(L_Z + 16384 <= L_T && L_T >= abuf(13) && L_T + 4 * C_TS <= L_ST && L_U >= abuf(14), "lds map");
constexpr int D_SLOT = 16384, N1 = 48, N2 = 16, NT = N1 + N2;

template <int LO, int HI, int FIRST, int DRAIN, class Pub>
__device__ __forceinline__ void conv_part(LAS char* tp, LAS char* gp, LAS char* zp, bool last, Pub&& pub) {
  constexpr int NH = HI / 2 + 1;
  s16x8 Fh[NH];
#pragma unroll
  for (int n = 0; n < NH; ++n) Fh[n] = cat8(tr_read(tp + n * 256), tr_read(tp + n * 256 - 32));
  f32x4 z[HI - LO + 1];
#pragma unroll
  for (int i = 0; i <= HI - LO; ++i) z[i] = (f32x4){0.f, 0.f, 0.f, 0.f};
  float p1 = 0.f, p2 = 0.f, p3 = 0.f, yp = 0.f;
  static_for<HI + 1>([&](auto P_) {
    constexpr int p = P_.value;
    if constexpr (p == DRAIN) pub();
    const s16x8 G = cat8(tr_read(gp + p * 128), tr_read(gp + p * 128 + 32));
#pragma unroll
    for (int n = 0; p + 2 * n <= HI; ++n)
      if (p + 2 * n >= LO) z[p + 2 * n - LO] = mfma16(G, Fh[n], z[p + 2 * n - LO]);
    if constexpr (p >= LO) {
      const f32x4 t = z[p - LO];
      const float s3 = t[3];
      const float s2 = t[2] + dpp<0x13C>(last ? p3 : s3);
      const float s1 = t[1] + dpp<0x13C>(last ? p2 : s2);
      const float y = t[0] + dpp<0x13C>(last ? p1 : s1);
      p1 = s1; p2 = s2; p3 = s3;
      if constexpr (p >= FIRST && ((p - FIRST) & 1)) {
        const unsigned o = pk_bf16(yp, y);
        *(LAS u16*)(zp + (p - 1) * 512) = (u16)o;
        *(LAS u16*)(zp + p * 512) = (u16)(o >> 16);
      }
      yp = y;
    }
  });
}

__global__ __launch_bounds__(512, 2) void k_all(const u16* __restrict__ x, const u16* __restrict__ kk, const u16* __restrict__ vv,
                                                const u16* __restrict__ shk, const u16* __restrict__ ssmk, const u16* __restrict__ ssmd,
                                                const u16* __restrict__ w1, const u16* __restrict__ w3, const u16* __restrict__ w2,
                                                u16* __restrict__ out, char* __restrict__ ws) {
  extern __shared__ __attribute__((aligned(16))) char smem[];
  LAS char* sm = (LAS char*)smem;
  int tid = threadIdx.x, lane = tid & 63;
  const int wid = __builtin_amdgcn_readfirstlane(tid >> 6);
  const int bid = blockIdx.x, xcd = bid & 7, rr = bid >> 3;
  const int mb = (xcd & 1) * 4 + (rr & 3), nb = (xcd >> 1) * 8 + (rr >> 2);
  const int wm = wid & 3, wn = wid >> 2;
  int g = lane >> 4, fr = lane & 15, qq = fr >> 2, pp = lane & 3;
  const __amdgpu_buffer_rsrc_t rs = __builtin_amdgcn_make_buffer_rsrc(ws, 0, WS_END, 0x00020000);
  const unsigned lds0 = (unsigned)(size_t)sm;
  STAMP(0);

  const char* gA[4];
#pragma unroll
  for (int i = 0; i < 4; ++i) gA[i] = (const char*)x + (size_t)(mb * 256 + i * 64 + wid * 8) * (D_ * 2);
  const char* gG = (const char*)w1 + ((size_t)(wid * 4) * F_ + nb * 112) * 2;
  const char* gU = (const char*)w3 + ((size_t)(wid * 4) * F_ + nb * 112) * 2;
  const unsigned vA = (lane >> 3) * (D_ * 2) + (((lane & 7) ^ ((wid & 1) * 4 + g)) * 16);
  const unsigned vB = (lane / 14) * (F_ * 2) + (lane % 14) * 16;
  const unsigned lA = lds0 + wid * 1024, lB = lds0 + wid * U_GS;
  const unsigned long long m56 = 0x00ffffffffffffffull;
#define A_DMA(q, i) dma16<((q) >> 1) * 128, abuf((q) >> 1) + (((q) & 1) * 2 + (i)) * 8192>(gA[((q) & 1) * 2 + (i)], vA, lA)
#define W_DMA(slot, i) do { \
    if (i == 0) { dma16_m<(slot) * U_WS>(gG, vB, lB, m56); gG += 32 * F_ * 2; } \
    else { dma16_m<(slot) * U_WS + U_UP>(gU, vB, lB, m56); gU += 32 * F_ * 2; } } while (0)
#define G1_DMA() do { A_DMA(0, 0); A_DMA(0, 1); A_DMA(1, 0); A_DMA(1, 1); W_DMA(0, 0); W_DMA(0, 1); W_DMA(1, 0); W_DMA(1, 1); } while (0)
#define G2_DMA() do { A_DMA(2, 0); A_DMA(2, 1); A_DMA(3, 0); A_DMA(3, 1); W_DMA(2, 0); W_DMA(2, 1); A_DMA(4, 0); A_DMA(4, 1); W_DMA(3, 0); W_DMA(3, 1); } while (0)
  const int tb = bid >> 4, cb = bid & 15;
  f32x4 acc[4][7];
  G1_DMA();
#pragma unroll
  for (int i = 0; i < 28; ++i) { acc[i & 3][i >> 2] = (f32x4){0.f, 0.f, 0.f, 0.f}; OPAQUE(acc[i & 3][i >> 2]); }
  if (tid == 0) *(LAS unsigned*)(sm + L_CNT) = 0u;
  unsigned ra[2][2], rw[2];
  ra[0][0] = lds0 + L_A + (wm * 64 + fr) * 128 + ((g ^ (fr >> 1)) * 16);
  ra[0][1] = ra[0][0] ^ 64;
  ra[1][0] = ra[0][0] + 65536; ra[1][1] = ra[0][1] + 65536;
  rw[0] = lds0 + 2 * g * U_GS + qq * 224 + wn * 112 + pp * 8;
  rw[1] = lds0 + 2 * g * U_GS + qq * 224 + wn * 112 + 96 + (pp & 1) * 8 + (pp >> 1) * U_UP;
  OPAQUE(ra[0][0]); OPAQUE(ra[0][1]); OPAQUE(ra[1][0]); OPAQUE(ra[1][1]); OPAQUE(rw[0]); OPAQUE(rw[1]);
  SCHED0();
  G2_DMA();
  if (wid == 7) dma16_m<L_SK>((const char*)shk + cb * 512, lane * 16, lds0, 0xffffffffull);
#define U_RD(i, X, Wl, Wh, t) do { \
    if constexpr (i >= 2 && i < 6) X[i - 2] = *(LAS s16x8*)(size_t)(ra[((t) >> 1) % 3 == 1][(t) & 1] + (((t) >> 1) % 3 == 2 ? 32768 : 0) + (i - 2) * 2048); \
    else { constexpr int j_ = i < 2 ? i : i - 4, c_ = j_ >> 1; \
      LAS char* p_ = (LAS char*)(size_t)(rw[c_ == 6] + ((t) & 3) * U_WS + (c_ == 6 ? 0 : (c_ >> 1) * 32 + (c_ & 1) * U_UP) + (j_ & 1) * U_GS); \
      if constexpr (j_ & 1) Wh[c_] = tr_read(p_); else Wl[c_] = tr_read(p_); } } while (0)
  float cs[7] = {0.f, 0.f, 0.f, 0.f, 0.f, 0.f, 0.f};
  s16x8 X[2][4];
  s16x4 Wl[2][7], Wh[2][7];
  WAIT_V(12); BAR();
  STAMP(1);
  static_for<18>([&](auto I_) { constexpr int i = I_.value; U_RD(i, X[0], Wl[0], Wh[0], 0); });
#pragma unroll
  for (int c = 0; c < 7; ++c) colsum(cs[c], cat8(Wl[0][c], Wh[0][c]), wm);
  SCHED0(); WAIT_VL(10); BAR(); SCHED0();

  const int cch = wid & 3;
  unsigned tag = 0, fu = 0, dsk = 0;
  u32x4 sg;
#define U_DMA(s, i) do { \
    if constexpr ((s) < 27 && (i) < 2) A_DMA((s) + 5, (i)); \
    if constexpr ((s) == 27 && (i) < 2) { unsigned l_ = threadIdx.x & 63; OPAQUE(l_); \
      dma16<(i) * 1024, (i) * 1024 + L_T + 32>((const char*)ssmk + (size_t)(bid * 4 + cch) * 4096 + wn * 2048, l_ * 16, lds0 + cch * C_TS + wn * 2048); } \
    if constexpr ((s) < 28 && (i) >= 2) W_DMA((s) & 3, (i) - 2); } while (0)
  static_for<31>([&](auto S_c) {
    constexpr int s = S_c.value, b = s & 1;
    STEP_T(s);
    if constexpr (s < 4) touch_if<s & 7, 32, 7>(wid, (const char*)x + bid * 16384 + s * 4096, lds0 + L_JUNK);
    else if constexpr (s < 8) touch_if<s & 7, 32, 11>(wid, (const char*)kk + (tb * 128 + (s - 4) * 32) * 2048 + cb * 128, lds0 + L_JUNK);
    else if constexpr (s < 12) touch_if<s & 7, 32, 11>(wid, (const char*)vv + (tb * 128 + (s - 8) * 32) * 2048 + cb * 128, lds0 + L_JUNK);
    else if constexpr (s >= 14 && s < 18) touch_if<s & 7, 32, 7>(wid, (const char*)ssmk + bid * 16384 + (s - 14) * 4096, lds0 + L_JUNK);
    else if constexpr (s >= 18 && s < 25) touch_if<s & 7, 32, 7>(wid, (const char*)w2 + bid * 28672 + (s - 18) * 4096, lds0 + L_JUNK);
    if constexpr (s == PSTEP + 4) flag_if(wid, ws + WS_FU + (cb * 16 + tb) * 128, tag);
    if constexpr (s == 28) {
      OPAQUE(tid);
      sg = LD_SC1(WS_ST + (mb * 256 + (tid & 255)) * 16);
      fu = __hip_atomic_load((GAS unsigned*)(ws + WS_FU + ((bid >> 4) * 16 + (tid & 15)) * 128), RLX);
      dsk = __hip_atomic_load((GAS u16*)(ssmd + bid * 4 + (tid & 3)), RLX);
    }
    if constexpr (s != PSTEP) {
      SCHED0();
      static_for<28>([&](auto I_) { constexpr int i = I_.value, c = i >> 2, m = i & 3;
        mfma_acc(acc[m][c], cat8(Wl[b][c], Wh[b][c]), X[b][m]);
        if constexpr (i < 18) U_RD(i, X[b ^ 1], Wl[b ^ 1], Wh[b ^ 1], s + 1);
        if constexpr ((i & 3) == 3 && i >= 7 && i < 20) U_DMA(s, (i - 7) >> 2);
        if constexpr (i >= 21) colsum(cs[i - 21], cat8(Wl[b ^ 1][i - 21], Wh[b ^ 1][i - 21]), wm);
        SCHED0(); });
    } else {
      OPAQUE(tid);
      const int cg = tid & 7, t0 = tb * 128 + (tid >> 3) * 2, c0 = cb * 64 + cg * 8, row = bid * 8 + wid;
      u32x4 kr[5], vr[2], sk[4], d[2];
      const unsigned ep = __hip_atomic_load((GAS unsigned*)(ws + WS_EP), RLX);
#pragma unroll
      for (int i = 0; i < 2; ++i) d[i] = *(const u32x4*)(x + (size_t)row * D_ + ((tid & 63) + 64 * i) * 8);
#pragma unroll
      for (int i = 0; i < 5; ++i) { const int t = t0 - 3 + i; kr[i] = *(const u32x4*)(kk + (size_t)(t < 0 ? 0 : t) * D_ + c0); }
#pragma unroll
      for (int i = 0; i < 2; ++i) vr[i] = *(const u32x4*)(vv + (size_t)(t0 + i) * D_ + c0);
      SCHED0();
      static_for<28>([&](auto I_) { constexpr int i = I_.value, c = i >> 2, m = i & 3;
        mfma_acc(acc[m][c], cat8(Wl[b][c], Wh[b][c]), X[b][m]);
        SCHED0(); });
#pragma unroll
      for (int i = 0; i < 4; ++i) sk[i] = *(LAS u32x4*)(sm + L_SK + cg * 64 + i * 16);
      static_for<18>([&](auto I_) { constexpr int i = I_.value; U_RD(i, X[b ^ 1], Wl[b ^ 1], Wh[b ^ 1], s + 1); });
      SCHED0();
      WAIT_V0C();
      static_for<4>([&](auto I_) { U_DMA(s, I_.value); });
      SCHED0();
      tag = __builtin_amdgcn_readfirstlane(ep) + 0x9E3779B9u;
      float sum = 0.f;
#pragma unroll
      for (int i = 0; i < 8; ++i) sum = dot2(d[i >> 2][i & 3], 0x3f803f80u, sum);
      const float mean = sum64(sum) * (1.0f / D_);
      float v0 = 0.f, v1 = 0.f;
#pragma unroll
      for (int i = 0; i < 8; ++i) { const float p = bflo(d[i >> 2][i & 3]) - mean, q = bfhi(d[i >> 2][i & 3]) - mean; v0 = fmaf(p, p, v0); v1 = fmaf(q, q, v1); }
      const float var = sum64(v0 + v1) * (1.0f / D_);
      const float sb = rbf(sqrtf(rbf(rbf(var) + 1e-5f)));
      ST_SC1(((u32x4){__float_as_uint(rbf(mean)), tag, __float_as_uint(sb), tag}), (tid & 63) == 0 ? WS_ST + row * 16 : WS_END);
#pragma unroll
      for (int i = 0; i < 3; ++i)
        if (t0 - 3 + i < 0) kr[i] = (u32x4){0u, 0u, 0u, 0u};
#pragma unroll
      for (int hh = 0; hh < 2; ++hh) {
        float u[2][4];
#pragma unroll
        for (int jj = 0; jj < 4; ++jj) {
          const int j = hh * 4 + jj, e = j >> 1;
          const unsigned sel = (j & 1) ? 0x07060302u : 0x05040100u;
          unsigned P[4];
#pragma unroll
          for (int i = 0; i < 4; ++i) P[i] = __builtin_amdgcn_perm(kr[i][e], kr[i + 1][e], sel);
#pragma unroll
          for (int p = 0; p < 2; ++p)
            u[p][jj] = dot2(P[p + 2], sk[e][(j & 1) * 2], dot2(P[p], sk[e][(j & 1) * 2 + 1], 0.f)) * ((j & 1) ? bfhi(vr[p][e]) : bflo(vr[p][e]));
        }
        ST_SC1(((u32x4){pk_bf16(u[0][0], u[0][1]), pk_bf16(u[0][2], u[0][3]), pk_bf16(u[1][0], u[1][1]), pk_bf16(u[1][2], u[1][3])}),
               WS_U + ((cb * 16 + cg * 2 + hh) * S_ + t0) * 8);
        ST_SC1(((u32x4){~0u, ~0u, ~0u, ~0u}), WS_EX + bid * 16384 + hh * 8192 + tid * 16);
      }
#pragma unroll
      for (int c = 0; c < 7; ++c) colsum(cs[c], cat8(Wl[b ^ 1][c], Wh[b ^ 1][c]), wm);
      SCHED0();
    }
    if constexpr (s < 30) {
      constexpr int n = s == PSTEP ? 9 : s == PSTEP + 1 ? 13 : s == PSTEP + 2 ? 13 : s == 28 ? 7 : s == 29 ? 3 : 8;
      WAIT_VL(n); BAR(); SCHED0();
    }
  });
  STEP_T(31);
  OPAQUE(tid); lane = tid & 63; g = lane >> 4; fr = lane & 15; qq = fr >> 2; pp = lane & 3;
  u32x4 ur[2];
  f32x4 cv[7];
  f32x2 st[4];
  {
    float r[8];
    SCHED0();
    static_for<28>([&](auto I_) { constexpr int i = I_.value, c = i >> 2, m = i & 3;
      mfma_acc(acc[m][c], cat8(Wl[1][c], Wh[1][c]), X[1][m]);
      if constexpr (i == 1) {
        *(LAS float*)(sm + L_CSP + wid * 512 + lane * 4) = add16(add32(cs[0], cs[1]), add32(cs[2], cs[3]));
        *(LAS float*)(sm + L_CSP + wid * 512 + 256 + lane * 4) = add16(add32(cs[4], cs[5]), add32(cs[6], 0.f));
      }
      if constexpr (i == 4) {
        for (unsigned spins = 0; !__all(sg[1] == tag && sg[3] == tag) && spins < SPIN_CAP; ++spins) {
          __builtin_amdgcn_s_sleep(2); FENCE_C();
          sg = LD_SC1(WS_ST + (mb * 256 + (tid & 255)) * 16);
        }
        *(LAS u32x2*)(sm + L_ST + (tid & 255) * 8) = (u32x2){sg[0], sg[2]};
        wait_flags((GAS unsigned*)(ws + WS_FU + ((bid >> 4) * 16 + fr) * 128), fu, tag);
#pragma unroll
        for (int k = 0; k < 2; ++k) ur[k] = LD_SC1(WS_U + bid * 16384 + tid * 32 + k * 16);
      }
      if constexpr (i == 10) {
        WAIT_L0(); BAR();
#pragma unroll
        for (int k = 0; k < 8; ++k) r[k] = *(LAS float*)(sm + L_CSP + wn * 2048 + k * 256 + lane * 4);
#pragma unroll
        for (int k = 0; k < 4; ++k) st[k] = *(LAS f32x2*)(sm + L_ST + (wm * 64 + k * 16 + fr) * 8);
      }
      if constexpr (i == 16) {
        LAS char* q = sm + L_CSF + wid * 512;
        *(LAS float*)(q + (((g & 1) << 1) | (g >> 1)) * 64 + fr * 4) = (r[0] + r[2]) + (r[4] + r[6]);
        *(LAS float*)(q + (((g & 1) << 1) | (g >> 1)) * 64 + 256 + fr * 4) = (r[1] + r[3]) + (r[5] + r[7]);
#pragma unroll
        for (int c2 = 0; c2 < 7; ++c2) cv[c2] = *(LAS f32x4*)(q + c2 * 64 + g * 16);
      }
      if constexpr (i == 20) {
        if (tid < 64) *(LAS u32x4*)(sm + L_U + g * C_US + fr * 16) = (u32x4){0u, 0u, 0u, 0u};
        else if (tid < 72) *(LAS u32x4*)(sm + L_T + (tid & 3) * C_TS + (tid & 4) * 4) = (u32x4){0u, 0u, 0u, 0u};
        else if (tid >= 128 && tid < 132) {
          LAS u16* p = (LAS u16*)(sm + L_T + (tid & 3) * C_TS + 32);
          *p = (u16)pk_bf16(bflo(*p) + bflo(dsk), 0.f);
        }
      }
      SCHED0(); });
  }
  MFMA_DONE();
  STEP_T(32);
  STAMP(2);

  {
    float ea[4], s2[4];
#pragma unroll
    for (int m = 0; m < 4; ++m) { ea[m] = -1.44269504089f * __builtin_amdgcn_rcpf(st[m][1]); s2[m] = st[m][1] * st[m][1]; }
    auto sw = [](f32x2 a, f32x2 b, float e, float q) {
      const f32x2 t = a * e;
      const f32x2 d = (f32x2){__builtin_amdgcn_exp2f(t[0]), __builtin_amdgcn_exp2f(t[1])} * q + q;
      const f32x2 h = a * b * (f32x2){__builtin_amdgcn_rcpf(d[0]), __builtin_amdgcn_rcpf(d[1])};
      return pk_bf16(h[0], h[1]);
    };
    const unsigned hb = WS_H + ((nb * 14 + wn * 7) * S_ + mb * 256 + wm * 64) * 16;
    const unsigned ho = ((g >> 1) * S_ + (g & 1) * 16 + fr) * 16;
#pragma unroll
    for (int P = 0; P < 3; ++P)
#pragma unroll
      for (int mp = 0; mp < 2; ++mp) {
        u32x2 h[2];
#pragma unroll
        for (int i = 0; i < 2; ++i) {
          const int m = 2 * mp + i;
          const f32x4 ga = acc[m][2 * P] - st[m][0] * cv[2 * P], up = acc[m][2 * P + 1] - st[m][0] * cv[2 * P + 1];
          h[i] = (u32x2){sw(ga.xy, up.xy, ea[m], s2[m]), sw(ga.zw, up.zw, ea[m], s2[m])};
        }
        ST_SC1(swap16(h[0], h[1]), hb + ho + (2 * P * S_ + mp * 32) * 16);
        SCHED0();
      }
    u32x2 t6[2];
#pragma unroll
    for (int mp = 0; mp < 2; ++mp) {
      const float e = lane < 32 ? ea[2 * mp] : ea[2 * mp + 1], q = lane < 32 ? s2[2 * mp] : s2[2 * mp + 1];
      f32x4 ga = acc[2 * mp][6] - st[2 * mp][0] * cv[6], up = acc[2 * mp + 1][6] - st[2 * mp + 1][0] * cv[6];
#pragma unroll
      for (int j = 0; j < 4; ++j) {
        auto r = __builtin_amdgcn_permlane32_swap(__float_as_uint(ga[j]), __float_as_uint(up[j]), false, false);
        ga[j] = __uint_as_float(r[0]); up[j] = __uint_as_float(r[1]);
      }
      t6[mp] = (u32x2){sw(ga.xy, up.xy, e, q), sw(ga.zw, up.zw, e, q)};
    }
    ST_SC1(swap16(t6[0], t6[1]), hb + (6 * S_ + ((g & 1) * 2 + (g >> 1)) * 16 + fr) * 16);
  }
#pragma unroll
  for (int c = 0; c < 4; ++c) {
    const unsigned sel = (c & 1) ? 0x07060302u : 0x05040100u;
    *(LAS u32x2*)(sm + L_U + c * C_US + 256 + tid * 8) =
        (u32x2){__builtin_amdgcn_perm(ur[0][2 + (c >> 1)], ur[0][c >> 1], sel), __builtin_amdgcn_perm(ur[1][2 + (c >> 1)], ur[1][c >> 1], sel)};
  }
  WAIT_L0(); BAR();
  STAMP(3);

  LAS char* tp = sm + L_T + cch * C_TS + 32 + (-12 + 4 * pp + 4 * (31 - 8 * g - qq)) * 2;
  LAS char* gp = sm + L_U + cch * C_US + 256 + (16 * pp - 112 + 4 * (8 * g + qq)) * 2;
  LAS char* zp = sm + L_Z + lane * 8 + cch * 2;
  auto pub_h = [&]() {
    WAIT_V(0);
    if (lane == 0) {
      if (__hip_atomic_fetch_add((LAS unsigned*)(sm + L_CNT), 1u, __ATOMIC_RELAXED, __HIP_MEMORY_SCOPE_WORKGROUP) == 7u)
        __hip_atomic_store((GAS unsigned*)(ws + WS_FH + (mb * 32 + nb) * 128), tag, RLX);
    }
  };
  const int kh = xcd >> 2, mb2 = mb * 2 + ((rr >> 2) & 1), nb2 = ((xcd >> 1) & 1) * 4 + (rr >> 3), pbid = bid ^ 4;

  if (wid >= 4) {
    __builtin_amdgcn_s_setprio(1);
    conv_part<CSPLIT - 1, 31, CSPLIT, 16>(tp, gp, zp, lane == 63, pub_h);
    __builtin_amdgcn_s_setprio(0);
    STAMP(4);
    const int lw = wid & 1;
    if (wid < 6) {
      GAS unsigned* fp = (GAS unsigned*)(ws + (lane < 19 ? WS_FH + (mb * 32 + kh * 13 + lane) * 128 : WS_FU + ((pbid & 15) * 16 + (pbid >> 4)) * 128));
      wait_flags(fp, __hip_atomic_load(fp, RLX), tag);
      STAMP(5);
      const unsigned v0 = WS_H + (lw * 2 * S_ + mb2 * 128) * 16 + lane * 16;
      const unsigned vK = v0 + kh * 1024, vS = v0 + (kh ^ 1) * 1024, k0 = kh * NT * (4 * S_ * 16);
      LAS char* dA = sm + lw * 4096 + lane * 16;
      u32x4 R[8][4];
#define H_LD(t) do { const unsigned so_ = (t) < N1 ? k0 + (t) * (4 * S_ * 16) : (t) * (4 * S_ * 16); \
        _Pragma("unroll") for (int i = 0; i < ((t) < N1 ? 4 : 2); ++i) \
          R[(t) & 7][i] = __builtin_amdgcn_raw_buffer_load_b128(rs, i < 2 ? vK : vS, so_ + (i & 1) * (S_ * 16), 16); } while (0)
#define H_WR(t) do { _Pragma("unroll") for (int i = 0; i < ((t) < N1 ? 4 : 2); ++i) \
          *(LAS u32x4*)(dA + ((t) & 7) * D_SLOT + (i & 1) * 2048 + (i >> 1) * 1024) = R[(t) & 7][i]; } while (0)
      static_for<8>([&](auto I_) { H_LD(I_.value); });
      SCHED0(); WAIT_L0(); BAR(); SCHED0();
      H_WR(0); WAIT_L0(); H_LD(8);
      SCHED0(); BAR(); SCHED0();
      STAMP(6);
      static_for<NT>([&](auto I_) { constexpr int s = I_.value;
        if constexpr (s + 1 < NT) { H_WR(s + 1); WAIT_L0(); }
        if constexpr (s + 9 < NT) H_LD(s + 9);
        SCHED0(); BAR(); SCHED0();
      });
    } else {
      const char* wB = (const char*)w2 + ((size_t)(lw * 16) * D_ + nb2 * 128) * 2;
      const char* gB = wB + (size_t)(kh * NT) * (32 * D_ * 2);
      const unsigned v2 = g * (D_ * 2) + ((fr ^ (g << 1)) * 16), v3 = (v2 ^ 128) + 8 * D_ * 2;
      const unsigned dB = lds0 + 8192 + lw * 4096;
#define D_STAGE(slot) do { const unsigned d_ = dB + (slot) * D_SLOT; \
        dma16<0, 0>(gB, v2, d_); dma16<0, 1024>(gB + 4 * D_ * 2, v2, d_); dma16<0, 2048>(gB, v3, d_); dma16<0, 3072>(gB + 4 * D_ * 2, v3, d_); \
        gB += 32 * D_ * 2; } while (0)
#pragma unroll
      for (int s = 0; s < 6; ++s) D_STAGE(s);
      WAIT_L0(); BAR();
      WAIT_V(20); BAR();
      D_STAGE(6); D_STAGE(7);
      for (int s = 0; s < NT - 8; ++s) {
        WAIT_V(24); BAR();
        if (s == N1 - 8) gB = wB + (size_t)N1 * (32 * D_ * 2);
        D_STAGE(s & 7);
      }
      static_for<8>([&](auto I_) { WAIT_V(I_.value < 6 ? 24 - 4 * I_.value : 0); BAR(); });
    }
    if (bid == 0 && wid == 7) {
#pragma unroll
      for (int i = 0; i < 4; ++i) { GAS unsigned* fp = (GAS unsigned*)(ws + WS_FU + (i * 64 + lane) * 128); wait_flags(fp, __hip_atomic_load(fp, RLX), tag); }
      if (lane == 0) __hip_atomic_store((GAS unsigned*)(ws + WS_EP), tag, RLX);
    }
    STAMP_OUT();
    return;
  }

  conv_part<0, CSPLIT - 1, 0, 6>(tp, gp, zp, lane == 63, pub_h);
  STAMP(4);
  WAIT_L0(); BAR();
  STAMP(5);
  {
    u32x4 o[4];
#pragma unroll
    for (int i = 0; i < 4; ++i) o[i] = *(LAS u32x4*)(sm + L_Z + i * 4096 + tid * 16);
#pragma unroll
    for (int i = 0; i < 4; ++i) ST_SC1(o[i], WS_Z + bid * 16384 + i * 4096 + tid * 16);
  }
  const int vm = wid & 1, vn = wid >> 1;
  unsigned rb[2][5];
  rb[0][4] = lds0 + g * 2048 + (vm * 32 + fr) * 16;
#pragma unroll
  for (int c = 0; c < 4; ++c) rb[0][c] = lds0 + 8192 + (8 * g + qq) * 256 + (((vn * 4 + c) ^ (qq | ((g & 1) << 2))) * 32) + pp * 8;
#pragma unroll
  for (int i = 0; i < 5; ++i) { rb[1][i] = rb[0][i] + 4 * D_SLOT; OPAQUE(rb[0][i]); OPAQUE(rb[1][i]); }
#define D_RD(i, b, slot, NM) do { \
    constexpr int so_ = ((slot) & 3) * D_SLOT; \
    if constexpr (i >= 2 && i < 2 + NM) X[b][i - 2] = *(LAS s16x8*)(size_t)(rb[(slot) >> 2][4] + so_ + ((i - 2) >> 1) * 1024 + ((i - 2) & 1) * 256); \
    else { constexpr int j_ = i < 2 ? i : i - NM, c_ = j_ >> 1; \
      LAS char* p_ = (LAS char*)(size_t)(rb[(slot) >> 2][c_] + so_ + (j_ & 1) * 1024); \
      if constexpr (j_ & 1) Wh[b][c_] = tr_read(p_); else Wl[b][c_] = tr_read(p_); } } while (0)
#define D_STEP(s, NM, NMN) do { constexpr int b_ = (s) & 1; \
    static_for<4 * NM>([&](auto I_) { constexpr int i = I_.value, c = i / NM, m = i % NM; \
      mfma_acc(ac[m][c], cat8(Wl[b_][c], Wh[b_][c]), X[b_][m]); \
      if constexpr (NM == 4) { if constexpr (i < 8 + NMN) D_RD(i, b_ ^ 1, ((s) + 1) & 7, NMN); } \
      else if constexpr (NMN != 0) { if constexpr (i < 2) D_RD(2 * i, b_ ^ 1, ((s) + 1) & 7, NMN); D_RD((i < 2 ? 2 * i + 1 : i + 2), b_ ^ 1, ((s) + 1) & 7, NMN); } \
      SCHED0(); }); } while (0)
  f32x4 ac[4][4];
#pragma unroll
  for (int m = 0; m < 4; ++m)
#pragma unroll
    for (int c = 0; c < 4; ++c) ac[m][c] = (f32x4){0.f, 0.f, 0.f, 0.f};
  const int row0 = mb2 * 128 + kh * 64 + vm * 32 + fr;
  GAS unsigned* fzp = (GAS unsigned*)(ws + WS_FZ + (nb2 * 32 + vn * 16 + fr) * 128);
  unsigned fz = 0;
  u32x4 xv[2][2];
  u32x2 zv[2][4];
#pragma unroll
  for (int m = 0; m < 2; ++m)
#pragma unroll
    for (int c = 0; c < 4; ++c) zv[m][c] = (u32x2){0u, 0u};
#pragma unroll
  for (int m = 0; m < 2; ++m)
#pragma unroll
    for (int P = 0; P < 2; ++P) xv[m][P] = *(const u32x4*)(x + (size_t)(row0 + m * 16) * D_ + nb2 * 128 + vn * 64 + (2 * P + (g & 1)) * 16 + (g >> 1) * 8);
  WAIT_L0(); BAR();
  STAMP(6);
  static_for<12>([&](auto I_) { constexpr int i = I_.value; D_RD(i, 0, 0, 4); });
  for (int it = 0; it < N1 / 8; ++it) {
    SCHED0(); WAIT_L0();
    if (it == 1) WAIT_V(0);
    BAR();
    if (it == 1 && tid == 0) __hip_atomic_store((GAS unsigned*)(ws + WS_FZ + bid * 128), tag, RLX);
    if (it == N1 / 8 - 2) fz = __hip_atomic_load(fzp, RLX);
    if (it == N1 / 8 - 1) {
      wait_flags(fzp, fz, tag);
#pragma unroll
      for (int m = 0; m < 2; ++m)
#pragma unroll
        for (int c = 0; c < 4; ++c)
          zv[m][c] = __builtin_amdgcn_raw_buffer_load_b64(rs, WS_Z + ((nb2 * 32 + vn * 16 + c * 4 + g) * S_ + row0 + m * 16) * 8, 0, 16);
    }
    SCHED0();
    D_STEP(0, 4, 4);
    static_for<7>([&](auto J_) { constexpr int s = J_.value + 1;
      WAIT_L0(); BAR(); SCHED0();
      D_STEP(s, 4, 4);
    });
  }
  MFMA_DONE();
  STAMP(7);
#pragma unroll
  for (int m = 0; m < 2; ++m)
#pragma unroll
    for (int P = 0; P < 2; ++P) {
      u32x4 o;
#pragma unroll
      for (int i = 0; i < 4; ++i) {
        const f32x4 a = ac[2 + m][2 * P + (i >> 1)];
        o[i] = min(pk_bf16(a[(i & 1) * 2], a[(i & 1) * 2 + 1]), 0xfffffffeu);
      }
      ST_SC1(o, WS_EX + pbid * 16384 + (m * 2 + P) * 4096 + tid * 16);
    }
  u32x4 pv[2][2];
  static_for<N2>([&](auto J_) { constexpr int s = J_.value;
    SCHED0(); WAIT_L0(); BAR();
    if constexpr (s == N2 - 3) {
#pragma unroll
      for (int i = 0; i < 4; ++i) pv[i >> 1][i & 1] = LD_SC1(WS_EX + bid * 16384 + i * 4096 + tid * 16);
    }
    SCHED0();
    D_STEP(s, 2, (s + 1 < N2 ? 2 : 0));
  });
  MFMA_DONE();
  STAMP(8);
  for (unsigned spins = 0; spins < SPIN_CAP; ++spins) {
    unsigned mx = 0;
#pragma unroll
    for (int i = 0; i < 16; ++i) mx = max(mx, pv[i >> 3][(i >> 2) & 1][i & 3]);
    if (!__any(mx == ~0u)) break;
    __builtin_amdgcn_s_sleep(1); FENCE_C();
#pragma unroll
    for (int i = 0; i < 4; ++i) pv[i >> 1][i & 1] = LD_SC1(WS_EX + bid * 16384 + i * 4096 + tid * 16);
  }
  STAMP(9);
#pragma unroll
  for (int m = 0; m < 2; ++m) {
    const f32x2 sv = *(LAS f32x2*)(sm + L_ST + ((row0 + m * 16) & 255) * 8);
    const float ri = __builtin_amdgcn_rcpf(sv[1]), mo = -sv[0] * ri;
#pragma unroll
    for (int P = 0; P < 2; ++P) {
      const u32x4 xs = swap16((u32x2){xv[m][P][0], xv[m][P][1]}, (u32x2){xv[m][P][2], xv[m][P][3]});
      u32x2 o[2];
#pragma unroll
      for (int i = 0; i < 4; ++i) {
        const int c = 2 * P + (i >> 1), d = i & 1;
        const float x0 = bflo(xs[i]), x1 = bfhi(xs[i]);
        const float y0 = fmaf(fmaf(x0, ri, mo), bflo(zv[m][c][d]), x0) + (ac[m][c][2 * d] + bflo(pv[m][P][i]));
        const float y1 = fmaf(fmaf(x1, ri, mo), bfhi(zv[m][c][d]), x1) + (ac[m][c][2 * d + 1] + bfhi(pv[m][P][i]));
        o[i >> 1][d] = pk_bf16(y0, y1);
      }
      *(u32x4*)(out + (size_t)(row0 + m * 16) * D_ + nb2 * 128 + vn * 64 + (2 * P + (g & 1)) * 16 + (g >> 1) * 8) = swap16(o[0], o[1]);
    }
  }
  STAMP(10);
  STAMP(11);
  STAMP_OUT();
}

extern "C" void kernel_launch(void* const* d_in, const int* in_sizes, int n_in, void* d_out, int out_size, void* d_ws, size_t ws_size,
                              hipStream_t stream) {
  (void)hipFuncSetAttribute((const void*)k_all, hipFuncAttributeMaxDynamicSharedMemorySize, L_ALLOC);
  k_all<<<256, 512, L_ALLOC, stream>>>((const u16*)d_in[0], (const u16*)d_in[1], (const u16*)d_in[2], (const u16*)d_in[3], (const u16*)d_in[4],
                                     (const u16*)d_in[5], (const u16*)d_in[7], (const u16*)d_in[8], (const u16*)d_in[9], (u16*)d_out, (char*)d_ws);
}
```

```cpp
#include <hip/hip_runtime.h>
#include <hip/hip_bf16.h>

#define LAS __attribute__((address_space(3)))
#define GAS __attribute__((address_space(1)))
typedef unsigned short u16;
typedef short s16x4 __attribute__((ext_vector_type(4)));
typedef short s16x8 __attribute__((ext_vector_type(8)));
typedef __bf16 bf16x2 __attribute__((ext_vector_type(2)));
typedef __bf16 bf16x8 __attribute__((ext_vector_type(8)));
typedef float f32x2 __attribute__((ext_vector_type(2)));
typedef float f32x4 __attribute__((ext_vector_type(4)));
typedef unsigned u32x4 __attribute__((ext_vector_type(4)));
typedef unsigned u32x2 __attribute__((ext_vector_type(2)));

constexpr int S_ = 2048, D_ = 1024, F_ = 3584;

constexpr unsigned WS_EP = 0;
constexpr unsigned WS_FU = 4096;
constexpr unsigned WS_FH = WS_FU + 32768;
constexpr unsigned WS_FZ = WS_FH + 32768;
constexpr unsigned WS_ST = WS_FZ + 32768;
constexpr unsigned WS_U = 1u << 20;
constexpr unsigned WS_Z = WS_U + S_ * D_ * 2;
constexpr unsigned WS_EX = WS_Z + S_ * D_ * 2;
constexpr unsigned WS_H = WS_EX + 256 * 16384;
constexpr unsigned WS_END = WS_H + S_ * F_ * 2;
constexpr unsigned SPIN_CAP = 1u << 18;

__device__ __forceinline__ float bflo(unsigned v) { return __uint_as_float(v << 16); }
__device__ __forceinline__ float bfhi(unsigned v) { return __uint_as_float(v & 0xffff0000u); }
__device__ __forceinline__ unsigned pk_bf16(float lo, float hi) {
  f32x2 v = {lo, hi};
  return __builtin_bit_cast(unsigned, __builtin_convertvector(v, bf16x2));
}
__device__ __forceinline__ float rbf(float x) { return bflo(pk_bf16(x, x)); }
__device__ __forceinline__ float dot2(unsigned a, unsigned b, float c) {
  return __builtin_amdgcn_fdot2_f32_bf16(__builtin_bit_cast(bf16x2, a), __builtin_bit_cast(bf16x2, b), c, false);
}
template <int CTRL> __device__ __forceinline__ float dpp(float v) {
  return __int_as_float(__builtin_amdgcn_update_dpp(0, __float_as_int(v), CTRL, 0xf, 0xf, true));
}
__device__ __forceinline__ float add32(float a, float b) {
  auto r = __builtin_amdgcn_permlane32_swap(__float_as_uint(a), __float_as_uint(b), false, false);
  return __uint_as_float(r[0]) + __uint_as_float(r[1]);
}
__device__ __forceinline__ float add16(float a, float b) {
  auto r = __builtin_amdgcn_permlane16_swap(__float_as_uint(a), __float_as_uint(b), false, false);
  return __uint_as_float(r[0]) + __uint_as_float(r[1]);
}
__device__ __forceinline__ float sum64(float v) {
  v += dpp<0xB1>(v); v += dpp<0x4E>(v); v += dpp<0x141>(v); v += dpp<0x140>(v);
  v = add16(v, v);
  return add32(v, v);
}
__device__ __forceinline__ u32x4 swap16(u32x2 a, u32x2 b) {
  auto r0 = __builtin_amdgcn_permlane16_swap(a[0], b[0], false, false);
  auto r1 = __builtin_amdgcn_permlane16_swap(a[1], b[1], false, false);
  return (u32x4){r0[0], r1[0], r0[1], r1[1]};
}
__device__ __forceinline__ f32x4 mfma16(s16x8 a, s16x8 b, f32x4 c) {
  return __builtin_amdgcn_mfma_f32_16x16x32_bf16(__builtin_bit_cast(bf16x8, a), __builtin_bit_cast(bf16x8, b), c, 0, 0, 0);
}
__device__ __forceinline__ void mfma_acc(f32x4& c, s16x8 a, s16x8 b) { asm volatile("v_mfma_f32_16x16x32_bf16 %0, %1, %2, %0" : "+v"(c) : "v"(a), "v"(b)); }
#define MFMA_DONE() do { asm volatile("s_nop 7\n\ts_nop 7"); __builtin_amdgcn_sched_barrier(0); } while (0)
__device__ __forceinline__ s16x4 tr_read(LAS char* p) { return __builtin_amdgcn_ds_read_tr16_b64_v4i16((LAS s16x4*)p); }
__device__ __forceinline__ s16x8 cat8(s16x4 a, s16x4 b) { return __builtin_shufflevector(a, b, 0, 1, 2, 3, 4, 5, 6, 7); }
template <int OFF, int DOFF = 0> __device__ __forceinline__ void dma16(const void* base, unsigned voff, unsigned dst) {
  asm volatile("s_add_u32 m0, %0, %4\n\ts_nop 0\n\tglobal_load_lds_dwordx4 %1, %2 offset:%3" ::"s"(dst), "v"(voff), "s"(base), "i"(OFF), "i"(DOFF - OFF) : "scc");
}
template <int DOFF> __device__ __forceinline__ void dma16_m(const void* base, unsigned voff, unsigned dst, unsigned long long mask) {
  asm volatile("s_add_u32 m0, %0, %4\n\ts_mov_b64 exec, %3\n\tglobal_load_lds_dwordx4 %1, %2\n\ts_mov_b64 exec, -1"
               ::"s"(dst), "v"(voff), "s"(base), "s"(mask), "i"(DOFF) : "scc");
}
#define TOUCH_MOD ""
#define HAUX 16
#define TMASK 31
template <int V, int N, int SH> __device__ __forceinline__ void touch_if(int sel, const void* base, unsigned dst) {
  unsigned t;
  asm volatile("s_cmp_lg_u32 %1, %4\n\ts_cbranch_scc1 1f\n\tv_mbcnt_lo_u32_b32 %0, -1, 0\n\tv_mbcnt_hi_u32_b32 %0, -1, %0\n\t"
               "v_min_u32 %0, %5, %0\n\tv_lshlrev_b32 %0, %6, %0\n\ts_mov_b32 m0, %3\n\ts_nop 0\n\tglobal_load_lds_dword %0, %2" TOUCH_MOD "\n1:"
               : "=&v"(t) : "s"(sel), "s"(base), "s"(dst), "i"(V), "i"(N - 1), "i"(SH) : "scc");
}
__device__ __forceinline__ void flag_if(int c, const void* p, unsigned val) {
  unsigned a, b;
  asm volatile("s_cmp_lg_u32 %2, 0\n\ts_cbranch_scc1 1f\n\tv_mov_b32 %0, 0\n\tv_mov_b32 %1, %4\n\ts_mov_b64 exec, 1\n\t"
               "global_store_dword %0, %1, %3 sc1\n\ts_mov_b64 exec, -1\n1:" : "=&v"(a), "=&v"(b) : "s"(c), "s"(p), "s"(val) : "scc", "memory");
}
__device__ __forceinline__ void colsum(float& cs, s16x8 w, int idx) {
  const u32x4 v = __builtin_bit_cast(u32x4, w);
  unsigned a, b;
  asm volatile("s_set_gpr_idx_on %3, gpr_idx(SRC1)\n\tv_lshlrev_b32 %1, 16, %4 ; CSCHK %4 %5\n\tv_and_b32 %2, 0xffff0000, %4\n\ts_set_gpr_idx_off\n\t"
               "v_add_f32 %0, %0, %1\n\tv_add_f32 %0, %0, %2" : "+v"(cs), "=&v"(a), "=&v"(b) : "s"(idx), "v"(v[0]), "v"(v));
}
#define OPAQUE(x) asm volatile("" : "+v"(x))
#define WAIT_V(n) asm volatile("s_waitcnt vmcnt(%0)" ::"i"(n) : "memory")
#define WAIT_L0() __builtin_amdgcn_s_waitcnt(0xC07F)
#define WAIT_V0C() __builtin_amdgcn_s_waitcnt(0x0F70)
#define WAIT_VL(n) do { WAIT_L0(); WAIT_V(n); } while (0)
#define FENCE_C() asm volatile("" ::: "memory")
#define BAR() do { FENCE_C(); __builtin_amdgcn_s_barrier(); FENCE_C(); } while (0)
#define SCHED0() __builtin_amdgcn_sched_barrier(0)
#define RLX __ATOMIC_RELAXED, __HIP_MEMORY_SCOPE_AGENT
#define ST_SC1(v, off) __builtin_amdgcn_raw_buffer_store_b128(v, rs, off, 0, 16)
#define LD_SC1(off) __builtin_amdgcn_raw_buffer_load_b128(rs, off, 0, 16)

#define STAMP(i)
#define STAMP_OUT()
#define STEP_T(idx)
template <int V> struct IC { static constexpr int value = V; };
template <int N, int I = 0, class Fn> __device__ __forceinline__ void static_for(Fn&& f) {
  if constexpr (I < N) { f(IC<I>{}); static_for<N, I + 1>(f); }
}

__device__ __forceinline__ void wait_flags(GAS unsigned* p, unsigned v, unsigned tag) {
  if (__builtin_expect(!__all(v == tag), 0)) {
    for (unsigned spins = 0; spins < SPIN_CAP; ++spins) {
      __builtin_amdgcn_s_sleep(2);
      if (__all(__hip_atomic_load(p, RLX) == tag)) break;
    }
  }
  FENCE_C();
}

constexpr int U_GS = 960;
constexpr int U_UP = 8 * U_GS + 16;
constexpr int U_WS = U_UP + 8 * U_GS;
constexpr int L_A = 61568;
constexpr int L_ST = L_A + 3 * 32768;
constexpr int L_JUNK = L_ST + 2048;
constexpr int L_SK = L_JUNK + 256;
constexpr int L_CNT = L_SK + 512;
constexpr int L_END = L_CNT + 16;
constexpr int L_ALLOC = L_END;
constexpr int abuf(int u) { return L_A + (u % 3 == 0 ? 0 : u % 3 == 1 ? 65536 : 32768); }
constexpr int PSTEP = 13;
constexpr bool has_touch(int s) { return s >= 0 && (s < 4 ? TMASK & 1 : s < 8 ? TMASK & 2 : s < 12 ? TMASK & 4 : s < 14 ? 0 : s < 18 ? TMASK & 8 : s < 25 ? TMASK & 16 : 0); }
constexpr int L_CSP = 0, L_CSF = 4096;
constexpr int C_US = 256 + 4096, L_U = 98304;
constexpr int L_Z = L_U + 4 * C_US;
constexpr int C_TS = 32 + 4096, L_T = 143232;
constexpr int CSPLIT = 22;
static_assert(L_Z + 16384 <= L_T && L_T >= abuf(13) && L_T + 4 * C_TS <= L_ST && L_U >= abuf(14), "lds map");
constexpr int D_SLOT = 8192, N1 = 48, N2 = 16, NT = N1 + N2;
constexpr int DRAIN_A = 6, DRAIN_B = 16;

template <int LO, int HI, int FIRST, class Hook>
__device__ __forceinline__ void conv_part(LAS char* tp, LAS char* gp, LAS char* zp, bool last, Hook&& hook) {
  constexpr int NH = HI / 2 + 1;
  s16x8 Fh[NH];
#pragma unroll
  for (int n = 0; n < NH; ++n) Fh[n] = cat8(tr_read(tp + n * 256), tr_read(tp + n * 256 - 32));
  f32x4 z[HI - LO + 1];
#pragma unroll
  for (int i = 0; i <= HI - LO; ++i) z[i] = (f32x4){0.f, 0.f, 0.f, 0.f};
  float p1 = 0.f, p2 = 0.f, p3 = 0.f, yp = 0.f;
  static_for<HI + 1>([&](auto P_) {
    constexpr int p = P_.value;
    hook(P_);
    const s16x8 G = cat8(tr_read(gp + p * 128), tr_read(gp + p * 128 + 32));
#pragma unroll
    for (int n = 0; p + 2 * n <= HI; ++n)
      if (p + 2 * n >= LO) z[p + 2 * n - LO] = mfma16(G, Fh[n], z[p + 2 * n - LO]);
    if constexpr (p >= LO) {
      const f32x4 t = z[p - LO];
      const float s3 = t[3];
      const float s2 = t[2] + dpp<0x13C>(last ? p3 : s3);
      const float s1 = t[1] + dpp<0x13C>(last ? p2 : s2);
      const float y = t[0] + dpp<0x13C>(last ? p1 : s1);
      p1 = s1; p2 = s2; p3 = s3;
      if constexpr (p >= FIRST && ((p - FIRST) & 1)) {
        const unsigned o = pk_bf16(yp, y);
        *(LAS u16*)(zp + (p - 1) * 512) = (u16)o;
        *(LAS u16*)(zp + p * 512) = (u16)(o >> 16);
      }
      yp = y;
    }
  });
}

__global__ __launch_bounds__(512, 2) void k_all(const u16* __restrict__ x, const u16* __restrict__ kk, const u16* __restrict__ vv,
                                                const u16* __restrict__ shk, const u16* __restrict__ ssmk, const u16* __restrict__ ssmd,
                                                const u16* __restrict__ w1, const u16* __restrict__ w3, const u16* __restrict__ w2,
                                                u16* __restrict__ out, char* __restrict__ ws) {
  extern __shared__ __attribute__((aligned(16))) char smem[];
  LAS char* sm = (LAS char*)smem;
  int tid = threadIdx.x, lane = tid & 63;
  const int wid = __builtin_amdgcn_readfirstlane(tid >> 6);
  const int bid = blockIdx.x, xcd = bid & 7, rr = bid >> 3;
  const int mb = (xcd & 1) * 4 + (rr & 3), nb = (xcd >> 1) * 8 + (rr >> 2);
  const int wm = wid & 3, wn = wid >> 2;
  int g = lane >> 4, fr = lane & 15, qq = fr >> 2, pp = lane & 3;
  const __amdgpu_buffer_rsrc_t rs = __builtin_amdgcn_make_buffer_rsrc(ws, 0, WS_END, 0x00020000);
  const unsigned lds0 = (unsigned)(size_t)sm;
  STAMP(0);

  const char* gA[4];
#pragma unroll
  for (int i = 0; i < 4; ++i) gA[i] = (const char*)x + (size_t)(mb * 256 + i * 64 + wid * 8) * (D_ * 2);
  const char* gG = (const char*)w1 + ((size_t)(wid * 4) * F_ + nb * 112) * 2;
  const char* gU = (const char*)w3 + ((size_t)(wid * 4) * F_ + nb * 112) * 2;
  const unsigned vA = (lane >> 3) * (D_ * 2) + (((lane & 7) ^ ((wid & 1) * 4 + g)) * 16);
  const unsigned vB = (lane / 14) * (F_ * 2) + (lane % 14) * 16;
  const unsigned lA = lds0 + wid * 1024, lB = lds0 + wid * U_GS;
  const unsigned long long m56 = 0x00ffffffffffffffull;
#define A_DMA(q, i) dma16<((q) >> 1) * 128, abuf((q) >> 1) + (((q) & 1) * 2 + (i)) * 8192>(gA[((q) & 1) * 2 + (i)], vA, lA)
#define W_DMA(slot, i) do { \
    if (i == 0) { dma16_m<(slot) * U_WS>(gG, vB, lB, m56); gG += 32 * F_ * 2; } \
    else { dma16_m<(slot) * U_WS + U_UP>(gU, vB, lB, m56); gU += 32 * F_ * 2; } } while (0)
#define G1_DMA() do { A_DMA(0, 0); A_DMA(0, 1); A_DMA(1, 0); A_DMA(1, 1); W_DMA(0, 0); W_DMA(0, 1); W_DMA(1, 0); W_DMA(1, 1); } while (0)
#define G2_DMA() do { A_DMA(2, 0); A_DMA(2, 1); A_DMA(3, 0); A_DMA(3, 1); W_DMA(2, 0); W_DMA(2, 1); A_DMA(4, 0); A_DMA(4, 1); W_DMA(3, 0); W_DMA(3, 1); } while (0)
  const int tb = bid >> 4, cb = bid & 15;
  f32x4 acc[4][7];
  G1_DMA();
#pragma unroll
  for (int i = 0; i < 28; ++i) { acc[i & 3][i >> 2] = (f32x4){0.f, 0.f, 0.f, 0.f}; OPAQUE(acc[i & 3][i >> 2]); }
  if (tid == 0) *(LAS unsigned*)(sm + L_CNT) = 0u;
  unsigned ra[2][2], rw[2];
  ra[0][0] = lds0 + L_A + (wm * 64 + fr) * 128 + ((g ^ (fr >> 1)) * 16);
  ra[0][1] = ra[0][0] ^ 64;
  ra[1][0] = ra[0][0] + 65536; ra[1][1] = ra[0][1] + 65536;
  rw[0] = lds0 + 2 * g * U_GS + qq * 224 + wn * 112 + pp * 8;
  rw[1] = lds0 + 2 * g * U_GS + qq * 224 + wn * 112 + 96 + (pp & 1) * 8 + (pp >> 1) * U_UP;
  OPAQUE(ra[0][0]); OPAQUE(ra[0][1]); OPAQUE(ra[1][0]); OPAQUE(ra[1][1]); OPAQUE(rw[0]); OPAQUE(rw[1]);
  SCHED0();
  G2_DMA();
  if (wid == 7) dma16_m<L_SK>((const char*)shk + cb * 512, lane * 16, lds0, 0xffffffffull);
#define U_RD(i, X, Wl, Wh, t) do { \
    if constexpr (i >= 2 && i < 6) X[i - 2] = *(LAS s16x8*)(size_t)(ra[((t) >> 1) % 3 == 1][(t) & 1] + (((t) >> 1) % 3 == 2 ? 32768 : 0) + (i - 2) * 2048); \
    else { constexpr int j_ = i < 2 ? i : i - 4, c_ = j_ >> 1; \
      LAS char* p_ = (LAS char*)(size_t)(rw[c_ == 6] + ((t) & 3) * U_WS + (c_ == 6 ? 0 : (c_ >> 1) * 32 + (c_ & 1) * U_UP) + (j_ & 1) * U_GS); \
      if constexpr (j_ & 1) Wh[c_] = tr_read(p_); else Wl[c_] = tr_read(p_); } } while (0)
  float cs[7] = {0.f, 0.f, 0.f, 0.f, 0.f, 0.f, 0.f};
  s16x8 X[2][4];
  s16x4 Wl[2][7], Wh[2][7];
  WAIT_V(12); BAR();
  STAMP(1);
  static_for<18>([&](auto I_) { constexpr int i = I_.value; U_RD(i, X[0], Wl[0], Wh[0], 0); });
#pragma unroll
  for (int c = 0; c < 7; ++c) colsum(cs[c], cat8(Wl[0][c], Wh[0][c]), wm);
  SCHED0(); WAIT_VL(10); BAR(); SCHED0();

  const int cch = wid & 3;
  unsigned tag = 0, fu = 0, dsk = 0;
  u32x4 sg;
#define U_DMA(s, i) do { \
    if constexpr ((s) < 27 && (i) < 2) A_DMA((s) + 5, (i)); \
    if constexpr ((s) == 27 && (i) < 2) { unsigned l_ = threadIdx.x & 63; OPAQUE(l_); \
      dma16<(i) * 1024, (i) * 1024 + L_T + 32>((const char*)ssmk + (size_t)(bid * 4 + cch) * 4096 + wn * 2048, l_ * 16, lds0 + cch * C_TS + wn * 2048); } \
    if constexpr ((s) < 28 && (i) >= 2) W_DMA((s) & 3, (i) - 2); } while (0)
  auto touch = [&](auto S_c) { constexpr int s = S_c.value;
    if constexpr (s < 4) { if constexpr (TMASK & 1) touch_if<s & 7, 32, 7>(wid, (const char*)x + bid * 16384 + ((s + rr) & 3) * 4096, lds0 + L_JUNK); }
    else if constexpr (s < 8) { if constexpr (TMASK & 2) touch_if<s & 7, 32, 11>(wid, (const char*)kk + (tb * 128 + (s - 4) * 32) * 2048 + cb * 128, lds0 + L_JUNK); }
    else if constexpr (s < 12) { if constexpr (TMASK & 4) touch_if<s & 7, 32, 11>(wid, (const char*)vv + (tb * 128 + (s - 8) * 32) * 2048 + cb * 128, lds0 + L_JUNK); }
    else if constexpr (s >= 14 && s < 18) { if constexpr (TMASK & 8) touch_if<s & 7, 32, 7>(wid, (const char*)ssmk + bid * 16384 + ((s + rr) & 3) * 4096, lds0 + L_JUNK); }
    else if constexpr (s >= 18 && s < 25) { if constexpr (TMASK & 16) touch_if<s & 7, 32, 7>(wid, (const char*)w2 + bid * 28672 + (s - 18) * 4096, lds0 + L_JUNK); }
  };
  static_for<31>([&](auto S_c) {
    constexpr int s = S_c.value, b = s & 1;
    STEP_T(s);
    if constexpr (s == PSTEP + 4) flag_if(wid, ws + WS_FU + (cb * 16 + tb) * 128, tag);
    if constexpr (s == 28) {
      OPAQUE(tid);
      sg = LD_SC1(WS_ST + (mb * 256 + (tid & 255)) * 16);
      fu = __hip_atomic_load((GAS unsigned*)(ws + WS_FU + ((bid >> 4) * 16 + (tid & 15)) * 128), RLX);
      dsk = __hip_atomic_load((GAS u16*)(ssmd + bid * 4 + (tid & 3)), RLX);
    }
    if constexpr (s != PSTEP) {
      SCHED0();
      static_for<28>([&](auto I_) { constexpr int i = I_.value, c = i >> 2, m = i & 3;
        mfma_acc(acc[m][c], cat8(Wl[b][c], Wh[b][c]), X[b][m]);
        if constexpr (i < 18) U_RD(i, X[b ^ 1], Wl[b ^ 1], Wh[b ^ 1], s + 1);
        if constexpr ((i & 3) == 3 && i >= 7 && i < 20) U_DMA(s, (i - 7) >> 2);
        if constexpr (i == 20) touch(S_c);
        if constexpr (i >= 21) colsum(cs[i - 21], cat8(Wl[b ^ 1][i - 21], Wh[b ^ 1][i - 21]), wm);
        SCHED0(); });
    } else {
      OPAQUE(tid);
      const int cg = tid & 7, t0 = tb * 128 + (tid >> 3) * 2, c0 = cb * 64 + cg * 8, row = bid * 8 + wid;
      u32x4 kr[5], vr[2], sk[4], d[2];
      const unsigned ep = __hip_atomic_load((GAS unsigned*)(ws + WS_EP), RLX);
#pragma unroll
      for (int i = 0; i < 2; ++i) d[i] = *(const u32x4*)(x + (size_t)row * D_ + ((tid & 63) + 64 * i) * 8);
#pragma unroll
      for (int i = 0; i < 5; ++i) { const int t = t0 - 3 + i; kr[i] = *(const u32x4*)(kk + (size_t)(t < 0 ? 0 : t) * D_ + c0); }
#pragma unroll
      for (int i = 0; i < 2; ++i) vr[i] = *(const u32x4*)(vv + (size_t)(t0 + i) * D_ + c0);
      SCHED0();
      static_for<28>([&](auto I_) { constexpr int i = I_.value, c = i >> 2, m = i & 3;
        mfma_acc(acc[m][c], cat8(Wl[b][c], Wh[b][c]), X[b][m]);
        SCHED0(); });
#pragma unroll
      for (int i = 0; i < 4; ++i) sk[i] = *(LAS u32x4*)(sm + L_SK + cg * 64 + i * 16);
      static_for<18>([&](auto I_) { constexpr int i = I_.value; U_RD(i, X[b ^ 1], Wl[b ^ 1], Wh[b ^ 1], s + 1); });
      SCHED0();
      WAIT_V0C();
      static_for<4>([&](auto I_) { U_DMA(s, I_.value); });
      SCHED0();
      tag = __builtin_amdgcn_readfirstlane(ep) + 0x9E3779B9u;
      float sum = 0.f;
#pragma unroll
      for (int i = 0; i < 8; ++i) sum = dot2(d[i >> 2][i & 3], 0x3f803f80u, sum);
      const float mean = sum64(sum) * (1.0f / D_);
      float v0 = 0.f, v1 = 0.f;
#pragma unroll
      for (int i = 0; i < 8; ++i) { const float p = bflo(d[i >> 2][i & 3]) - mean, q = bfhi(d[i >> 2][i & 3]) - mean; v0 = fmaf(p, p, v0); v1 = fmaf(q, q, v1); }
      const float var = sum64(v0 + v1) * (1.0f / D_);
      const float sb = rbf(sqrtf(rbf(rbf(var) + 1e-5f)));
      ST_SC1(((u32x4){__float_as_uint(rbf(mean)), tag, __float_as_uint(sb), tag}), (tid & 63) == 0 ? WS_ST + row * 16 : WS_END);
#pragma unroll
      for (int i = 0; i < 3; ++i)
        if (t0 - 3 + i < 0) kr[i] = (u32x4){0u, 0u, 0u, 0u};
#pragma unroll
      for (int hh = 0; hh < 2; ++hh) {
        float u[2][4];
#pragma unroll
        for (int jj = 0; jj < 4; ++jj) {
          const int j = hh * 4 + jj, e = j >> 1;
          const unsigned sel = (j & 1) ? 0x07060302u : 0x05040100u;
          unsigned P[4];
#pragma unroll
          for (int i = 0; i < 4; ++i) P[i] = __builtin_amdgcn_perm(kr[i][e], kr[i + 1][e], sel);
#pragma unroll
          for (int p = 0; p < 2; ++p)
            u[p][jj] = dot2(P[p + 2], sk[e][(j & 1) * 2], dot2(P[p], sk[e][(j & 1) * 2 + 1], 0.f)) * ((j & 1) ? bfhi(vr[p][e]) : bflo(vr[p][e]));
        }
        ST_SC1(((u32x4){pk_bf16(u[0][0], u[0][1]), pk_bf16(u[0][2], u[0][3]), pk_bf16(u[1][0], u[1][1]), pk_bf16(u[1][2], u[1][3])}),
               WS_U + ((cb * 16 + cg * 2 + hh) * S_ + t0) * 8);
        ST_SC1(((u32x4){~0u, ~0u, ~0u, ~0u}), WS_EX + bid * 16384 + hh * 8192 + tid * 16);
      }
#pragma unroll
      for (int c = 0; c < 7; ++c) colsum(cs[c], cat8(Wl[b ^ 1][c], Wh[b ^ 1][c]), wm);
      SCHED0();
    }
    if constexpr (s < 30) {
      constexpr int n = s == PSTEP ? 9 : s == PSTEP + 1 ? 13 : s == PSTEP + 2 ? 13 : s == 28 ? 7 : s == 29 ? 3 : 8;
      constexpr int tm = (has_touch(s) ? 1 << (s & 7) : 0) | (has_touch(s - 1) ? 1 << ((s - 1) & 7) : 0);
      WAIT_L0();
      if constexpr (tm != 0) asm volatile("s_waitcnt vmcnt(%0)\n\ts_bitcmp1_b32 %2, %3\n\ts_cbranch_scc1 1f\n\ts_waitcnt vmcnt(%1)\n1:" ::"i"(n + 1), "i"(n), "i"(tm), "s"(wid) : "scc", "memory");
      else WAIT_V(n);
      BAR(); SCHED0();
    }
  });
  STEP_T(31);
  OPAQUE(tid); lane = tid & 63; g = lane >> 4; fr = lane & 15; qq = fr >> 2; pp = lane & 3;
  u32x4 ur[2];
  f32x4 cv[7];
  f32x2 st[4];
  {
    float r[8];
    SCHED0();
    static_for<28>([&](auto I_) { constexpr int i = I_.value, c = i >> 2, m = i & 3;
      mfma_acc(acc[m][c], cat8(Wl[1][c], Wh[1][c]), X[1][m]);
      if constexpr (i == 1) {
        *(LAS float*)(sm + L_CSP + wid * 512 + lane * 4) = add16(add32(cs[0], cs[1]), add32(cs[2], cs[3]));
        *(LAS float*)(sm + L_CSP + wid * 512 + 256 + lane * 4) = add16(add32(cs[4], cs[5]), add32(cs[6], 0.f));
      }
      if constexpr (i == 4) {
        for (unsigned spins = 0; !__all(sg[1] == tag && sg[3] == tag) && spins < SPIN_CAP; ++spins) {
          __builtin_amdgcn_s_sleep(2); FENCE_C();
          sg = LD_SC1(WS_ST + (mb * 256 + (tid & 255)) * 16);
        }
        *(LAS u32x2*)(sm + L_ST + (tid & 255) * 8) = (u32x2){sg[0], sg[2]};
        wait_flags((GAS unsigned*)(ws + WS_FU + ((bid >> 4) * 16 + fr) * 128), fu, tag);
#pragma unroll
        for (int k = 0; k < 2; ++k) ur[k] = LD_SC1(WS_U + bid * 16384 + tid * 32 + k * 16);
      }
      if constexpr (i == 10) {
        WAIT_L0(); BAR();
#pragma unroll
        for (int k = 0; k < 8; ++k) r[k] = *(LAS float*)(sm + L_CSP + wn * 2048 + k * 256 + lane * 4);
#pragma unroll
        for (int k = 0; k < 4; ++k) st[k] = *(LAS f32x2*)(sm + L_ST + (wm * 64 + k * 16 + fr) * 8);
      }
      if constexpr (i == 16) {
        LAS char* q = sm + L_CSF + wid * 512;
        *(LAS float*)(q + (((g & 1) << 1) | (g >> 1)) * 64 + fr * 4) = (r[0] + r[2]) + (r[4] + r[6]);
        *(LAS float*)(q + (((g & 1) << 1) | (g >> 1)) * 64 + 256 + fr * 4) = (r[1] + r[3]) + (r[5] + r[7]);
#pragma unroll
        for (int c2 = 0; c2 < 7; ++c2) cv[c2] = *(LAS f32x4*)(q + c2 * 64 + g * 16);
      }
      if constexpr (i == 20) {
        if (tid < 64) *(LAS u32x4*)(sm + L_U + g * C_US + fr * 16) = (u32x4){0u, 0u, 0u, 0u};
        else if (tid < 72) *(LAS u32x4*)(sm + L_T + (tid & 3) * C_TS + (tid & 4) * 4) = (u32x4){0u, 0u, 0u, 0u};
        else if (tid >= 128 && tid < 132) {
          LAS u16* p = (LAS u16*)(sm + L_T + (tid & 3) * C_TS + 32);
          *p = (u16)pk_bf16(bflo(*p) + bflo(dsk), 0.f);
        }
      }
      SCHED0(); });
  }
  MFMA_DONE();
  STEP_T(32);
  STAMP(2);

  {
    float ea[4], s2[4];
#pragma unroll
    for (int m = 0; m < 4; ++m) { ea[m] = -1.44269504089f * __builtin_amdgcn_rcpf(st[m][1]); s2[m] = st[m][1] * st[m][1]; }
    auto sw = [](f32x2 a, f32x2 b, float e, float q) {
      const f32x2 t = a * e;
      const f32x2 d = (f32x2){__builtin_amdgcn_exp2f(t[0]), __builtin_amdgcn_exp2f(t[1])} * q + q;
      const f32x2 h = a * b * (f32x2){__builtin_amdgcn_rcpf(d[0]), __builtin_amdgcn_rcpf(d[1])};
      return pk_bf16(h[0], h[1]);
    };
    const unsigned hb = WS_H + ((nb * 14 + wn * 7) * S_ + mb * 256 + wm * 64) * 16;
    const unsigned ho = ((g >> 1) * S_ + (g & 1) * 16 + fr) * 16;
#pragma unroll
    for (int P = 0; P < 3; ++P)
#pragma unroll
      for (int mp = 0; mp < 2; ++mp) {
        u32x2 h[2];
#pragma unroll
        for (int i = 0; i < 2; ++i) {
          const int m = 2 * mp + i;
          const f32x4 ga = acc[m][2 * P] - st[m][0] * cv[2 * P], up = acc[m][2 * P + 1] - st[m][0] * cv[2 * P + 1];
          h[i] = (u32x2){sw(ga.xy, up.xy, ea[m], s2[m]), sw(ga.zw, up.zw, ea[m], s2[m])};
        }
        ST_SC1(swap16(h[0], h[1]), hb + ho + (2 * P * S_ + mp * 32) * 16);
        SCHED0();
      }
    u32x2 t6[2];
#pragma unroll
    for (int mp = 0; mp < 2; ++mp) {
      const float e = lane < 32 ? ea[2 * mp] : ea[2 * mp + 1], q = lane < 32 ? s2[2 * mp] : s2[2 * mp + 1];
      f32x4 ga = acc[2 * mp][6] - st[2 * mp][0] * cv[6], up = acc[2 * mp + 1][6] - st[2 * mp + 1][0] * cv[6];
#pragma unroll
      for (int j = 0; j < 4; ++j) {
        auto r = __builtin_amdgcn_permlane32_swap(__float_as_uint(ga[j]), __float_as_uint(up[j]), false, false);
        ga[j] = __uint_as_float(r[0]); up[j] = __uint_as_float(r[1]);
      }
      t6[mp] = (u32x2){sw(ga.xy, up.xy, e, q), sw(ga.zw, up.zw, e, q)};
    }
    ST_SC1(swap16(t6[0], t6[1]), hb + (6 * S_ + ((g & 1) * 2 + (g >> 1)) * 16 + fr) * 16);
  }
#pragma unroll
  for (int c = 0; c < 4; ++c) {
    const unsigned sel = (c & 1) ? 0x07060302u : 0x05040100u;
    *(LAS u32x2*)(sm + L_U + c * C_US + 256 + tid * 8) =
        (u32x2){__builtin_amdgcn_perm(ur[0][2 + (c >> 1)], ur[0][c >> 1], sel), __builtin_amdgcn_perm(ur[1][2 + (c >> 1)], ur[1][c >> 1], sel)};
  }
  WAIT_L0(); BAR();
  STAMP(3);

  LAS char* tp = sm + L_T + cch * C_TS + 32 + (-12 + 4 * pp + 4 * (31 - 8 * g - qq)) * 2;
  LAS char* gp = sm + L_U + cch * C_US + 256 + (16 * pp - 112 + 4 * (8 * g + qq)) * 2;
  LAS char* zp = sm + L_Z + lane * 8 + cch * 2;
  auto pub_h = [&]() {
    WAIT_V(0);
    if (lane == 0) {
      if (__hip_atomic_fetch_add((LAS unsigned*)(sm + L_CNT), 1u, __ATOMIC_RELAXED, __HIP_MEMORY_SCOPE_WORKGROUP) == 7u)
        __hip_atomic_store((GAS unsigned*)(ws + WS_FH + (mb * 32 + nb) * 128), tag, RLX);
    }
  };
  const int kh = xcd >> 2, mb2 = mb * 2 + ((rr >> 2) & 1), nb2 = ((xcd >> 1) & 1) * 4 + (rr >> 3), pbid = bid ^ 4;

  if (wid < 4) {
    const char* wB = (const char*)w2 + ((size_t)(wid * 8) * D_ + nb2 * 128) * 2;
    const char* gB = wB + (size_t)(kh * NT) * (32 * D_ * 2);
    const unsigned v2 = g * (D_ * 2) + ((fr ^ ((g | ((wid & 1) << 2)) << 1)) * 16);
    const unsigned dB = lds0 + wid * 2048;
#define D_STAGE(d, o) do { dma16<0, (o)>(gB, v2, d); dma16<0, (o) + 1024>(gB + 4 * D_ * 2, v2, d); gB += 32 * D_ * 2; } while (0)
#define D_GROUP(j) do { const unsigned d_ = dB + ((j) & 3) * (4 * D_SLOT); \
      D_STAGE(d_, 0); D_STAGE(d_, D_SLOT); D_STAGE(d_, 2 * D_SLOT); D_STAGE(d_, 3 * D_SLOT); } while (0)
    conv_part<0, CSPLIT - 1, 0>(tp, gp, zp, lane == 63, [&](auto P_) { constexpr int p = P_.value;
      if constexpr (p == DRAIN_A) pub_h();
      if constexpr (p > DRAIN_A && p <= DRAIN_A + 12) D_STAGE(dB, (p - DRAIN_A - 1) * D_SLOT); });
    STAMP(4);
    WAIT_VL(16); BAR();
    STAMP(5);
    {
      u32x4 o[4];
#pragma unroll
      for (int i = 0; i < 4; ++i) o[i] = *(LAS u32x4*)(sm + L_Z + i * 4096 + tid * 16);
#pragma unroll
      for (int i = 0; i < 4; ++i) ST_SC1(o[i], WS_Z + bid * 16384 + i * 4096 + tid * 16);
    }
    WAIT_VL(12); BAR();
    D_GROUP(3); D_GROUP(4);
    WAIT_V(20); BAR();
    D_GROUP(5);
    for (int j = 3; j < NT / 4 - 3; ++j) {
      WAIT_V(16); BAR();
      if (j == 3 && tid == 0) __hip_atomic_store((GAS unsigned*)(ws + WS_FZ + bid * 128), tag, RLX);
      if (j + 3 == N1 / 4) gB = wB + (size_t)N1 * (32 * D_ * 2);
      D_GROUP(j + 3);
    }
    WAIT_V(16); BAR();
    WAIT_V(8); BAR();
    WAIT_V(0); BAR();
    if (bid == 0 && wid == 3) {
#pragma unroll
      for (int i = 0; i < 4; ++i) { GAS unsigned* fp = (GAS unsigned*)(ws + WS_FU + (i * 64 + lane) * 128); wait_flags(fp, __hip_atomic_load(fp, RLX), tag); }
      if (lane == 0) __hip_atomic_store((GAS unsigned*)(ws + WS_EP), tag, RLX);
    }
    STAMP_OUT();
    return;
  }

  __builtin_amdgcn_s_setprio(1);
  conv_part<CSPLIT - 1, 31, CSPLIT>(tp, gp, zp, lane == 63, [&](auto P_) { if constexpr (P_.value == DRAIN_B) pub_h(); });
  STAMP(4);
  const int row0 = mb2 * 128 + kh * 64 + cch * 16 + fr;
  u32x4 xv[4];
#pragma unroll
  for (int P = 0; P < 4; ++P) xv[P] = *(const u32x4*)(x + (size_t)row0 * D_ + nb2 * 128 + (2 * P + (g & 1)) * 16 + (g >> 1) * 8);
  {
    GAS unsigned* fp = (GAS unsigned*)(ws + (lane < 19 ? WS_FH + (mb * 32 + kh * 13 + lane) * 128 : WS_FU + ((pbid & 15) * 16 + (pbid >> 4)) * 128));
    wait_flags(fp, __hip_atomic_load(fp, RLX), tag);
  }
  STAMP(5);
  const unsigned vK = WS_H + (g * S_ + row0) * 16, vS = vK ^ 1024;
  unsigned ko = kh * NT * (4 * S_ * 16);
  u32x4 Hf[8][2];
#define H_LD(t, m) Hf[(t) & 7][m] = __builtin_amdgcn_raw_buffer_load_b128(rs, (m) ? vS : vK, ko, 16)
  static_for<8>([&](auto I_) { H_LD(I_.value, 0); H_LD(I_.value, 1); ko += 4 * S_ * 16; });
  unsigned rb[2][8];
#pragma unroll
  for (int c = 0; c < 8; ++c) {
    rb[0][c] = lds0 + (8 * g + qq) * 256 + ((c ^ (qq | ((g & 1) << 2))) * 32) + pp * 8;
    rb[1][c] = rb[0][c] + 8 * D_SLOT; OPAQUE(rb[0][c]); OPAQUE(rb[1][c]);
  }
  s16x4 Vl[2][8], Vh[2][8];
#define D_RD(i, b, slot) do { LAS char* p_ = (LAS char*)(size_t)(rb[((slot) >> 3) & 1][(i) >> 1] + ((slot) & 7) * D_SLOT + ((i) & 1) * 1024); \
    if constexpr ((i) & 1) Vh[b][(i) >> 1] = tr_read(p_); else Vl[b][(i) >> 1] = tr_read(p_); } while (0)
#define D_STEP(s, NM, LD, RD) do { constexpr int b_ = (s) & 1; \
    if constexpr (((s) & 3) == 3 && RD) { WAIT_L0(); BAR(); } \
    SCHED0(); \
    static_for<8 * NM>([&](auto I_) { constexpr int i = I_.value, c = i & 7, m = i >> 3; \
      mfma_acc(ac[m][c], cat8(Vl[b_][c], Vh[b_][c]), __builtin_bit_cast(s16x8, Hf[(s) & 7][m])); \
      if constexpr (RD) { if constexpr (NM == 2) D_RD(i, b_ ^ 1, (s) + 1); else { D_RD(2 * i, b_ ^ 1, (s) + 1); D_RD(2 * i + 1, b_ ^ 1, (s) + 1); } } \
      if constexpr (c == 7 && m < LD) H_LD(s, m); \
      SCHED0(); }); \
    ko += 4 * S_ * 16; } while (0)
  f32x4 ac[2][8];
#pragma unroll
  for (int i = 0; i < 16; ++i) { ac[i >> 3][i & 7] = (f32x4){0.f, 0.f, 0.f, 0.f}; OPAQUE(ac[i >> 3][i & 7]); }
  GAS unsigned* fzp = (GAS unsigned*)(ws + WS_FZ + (nb2 * 32 + (lane & 31)) * 128);
  u32x2 zv[8];
  WAIT_L0(); BAR();
  STAMP(6);
  static_for<16>([&](auto I_) { D_RD(I_.value, 0, 0); });
  for (int it = 0; it < N1 / 16 - 1; ++it) static_for<16>([&](auto J_) { D_STEP(J_.value, 2, 2, true); });
  unsigned fz = __hip_atomic_load(fzp, RLX);
  static_for<16>([&](auto J_) { constexpr int s = J_.value;
    if constexpr (s == 8) {
      ko = N1 * (4 * S_ * 16);
      wait_flags(fzp, fz, tag);
#pragma unroll
      for (int c = 0; c < 8; ++c) zv[c] = __builtin_amdgcn_raw_buffer_load_b64(rs, WS_Z + ((nb2 * 32 + c * 4 + g) * S_ + row0) * 8, 0, 16);
    }
    D_STEP(s, 2, (s < 8 ? 2 : 1), true); });
  MFMA_DONE();
  STAMP(7);
#pragma unroll
  for (int P = 0; P < 4; ++P) {
    u32x4 o;
#pragma unroll
    for (int i = 0; i < 4; ++i) {
      const f32x4 a = ac[1][2 * P + (i >> 1)];
      o[i] = min(pk_bf16(a[(i & 1) * 2], a[(i & 1) * 2 + 1]), 0xfffffffeu);
    }
    ST_SC1(o, WS_EX + pbid * 16384 + P * 4096 + (tid & 255) * 16);
  }
  u32x4 pv[4];
  static_for<N2>([&](auto J_) { constexpr int s = J_.value;
    if constexpr (s == N2 - 3) {
#pragma unroll
      for (int i = 0; i < 4; ++i) pv[i] = LD_SC1(WS_EX + bid * 16384 + i * 4096 + (tid & 255) * 16);
    }
    D_STEP(s, 1, (s + 8 < N2 ? 1 : 0), (s + 1 < N2));
  });
  MFMA_DONE();
  STAMP(8);
  for (unsigned spins = 0; spins < SPIN_CAP; ++spins) {
    unsigned mx = 0;
#pragma unroll
    for (int i = 0; i < 16; ++i) mx = max(mx, pv[i >> 2][i & 3]);
    if (!__any(mx == ~0u)) break;
    __builtin_amdgcn_s_sleep(1); FENCE_C();
#pragma unroll
    for (int i = 0; i < 4; ++i) pv[i] = LD_SC1(WS_EX + bid * 16384 + i * 4096 + (tid & 255) * 16);
  }
  STAMP(9);
  {
    const f32x2 sv = *(LAS f32x2*)(sm + L_ST + (row0 & 255) * 8);
    const float ri = __builtin_amdgcn_rcpf(sv[1]), mo = -sv[0] * ri;
    const __amdgpu_buffer_rsrc_t ro = __builtin_amdgcn_make_buffer_rsrc(out, 0, S_ * D_ * 2, 0x00020000);
#pragma unroll
    for (int P = 0; P < 4; ++P) {
      const u32x4 xs = swap16((u32x2){xv[P][0], xv[P][1]}, (u32x2){xv[P][2], xv[P][3]});
      u32x2 o[2];
#pragma unroll
      for (int i = 0; i < 4; ++i) {
        const int c = 2 * P + (i >> 1), d = i & 1;
        const float x0 = bflo(xs[i]), x1 = bfhi(xs[i]);
        const float y0 = fmaf(fmaf(x0, ri, mo), bflo(zv[c][d]), x0) + (ac[0][c][2 * d] + bflo(pv[P][i]));
        const float y1 = fmaf(fmaf(x1, ri, mo), bfhi(zv[c][d]), x1) + (ac[0][c][2 * d + 1] + bfhi(pv[P][i]));
        o[i >> 1][d] = pk_bf16(y0, y1);
      }
      __builtin_amdgcn_raw_buffer_store_b128(swap16(o[0], o[1]), ro, (row0 * D_ + nb2 * 128 + (2 * P + (g & 1)) * 16 + (g >> 1) * 8) * 2, 0, 16);
    }
  }
  STAMP(10);
  STAMP(11);
  STAMP_OUT();
}

#define WS_OFF 0
extern "C" void kernel_launch(void* const* d_in, const int* in_sizes, int n_in, void* d_out, int out_size, void* d_ws, size_t ws_size,
                              hipStream_t stream) {
  (void)hipFuncSetAttribute((const void*)k_all, hipFuncAttributeMaxDynamicSharedMemorySize, L_ALLOC);
  k_all<<<256, 512, L_ALLOC, stream>>>((const u16*)d_in[0], (const u16*)d_in[1], (const u16*)d_in[2], (const u16*)d_in[3], (const u16*)d_in[4],
                                     (const u16*)d_in[5], (const u16*)d_in[7], (const u16*)d_in[8], (const u16*)d_in[9], (u16*)d_out, (char*)d_ws + WS_OFF);
}
```

```cpp
#include <hip/hip_runtime.h>
#include <hip/hip_bf16.h>

#define LAS __attribute__((address_space(3)))
#define GAS __attribute__((address_space(1)))
typedef unsigned short u16;
typedef short s16x4 __attribute__((ext_vector_type(4)));
typedef short s16x8 __attribute__((ext_vector_type(8)));
typedef __bf16 bf16x2 __attribute__((ext_vector_type(2)));
typedef __bf16 bf16x8 __attribute__((ext_vector_type(8)));
typedef float f32x2 __attribute__((ext_vector_type(2)));
typedef float f32x4 __attribute__((ext_vector_type(4)));
typedef unsigned u32x4 __attribute__((ext_vector_type(4)));
typedef unsigned u32x2 __attribute__((ext_vector_type(2)));

constexpr int S_ = 2048, D_ = 1024, F_ = 3584;

constexpr unsigned WS_EP = 0;
constexpr unsigned WS_FU = 4096;
constexpr unsigned WS_FH = WS_FU + 32768;
constexpr unsigned WS_FZ = WS_FH + 32768;
constexpr unsigned WS_ST = WS_FZ + 32768;
constexpr unsigned WS_U = 1u << 20;
constexpr unsigned WS_Z = WS_U + S_ * D_ * 2;
constexpr unsigned WS_EX = WS_Z + S_ * D_ * 2;
constexpr unsigned WS_H = WS_EX + 256 * 16384;
constexpr unsigned WS_END = WS_H + S_ * F_ * 2;
static_assert(WS_END <= 0x2000000, "offsets used to drop a store");
constexpr unsigned SPIN_CAP = 1u << 18;

__device__ __forceinline__ float bflo(unsigned v) { return __uint_as_float(v << 16); }
__device__ __forceinline__ float bfhi(unsigned v) { return __uint_as_float(v & 0xffff0000u); }
__device__ __forceinline__ unsigned pk_bf16(float lo, float hi) {
  f32x2 v = {lo, hi};
  return __builtin_bit_cast(unsigned, __builtin_convertvector(v, bf16x2));
}
__device__ __forceinline__ float rbf(float x) { return bflo(pk_bf16(x, x)); }
__device__ __forceinline__ float dot2(unsigned a, unsigned b, float c) {
  return __builtin_amdgcn_fdot2_f32_bf16(__builtin_bit_cast(bf16x2, a), __builtin_bit_cast(bf16x2, b), c, false);
}
template <int CTRL> __device__ __forceinline__ float dpp(float v) {
  return __int_as_float(__builtin_amdgcn_update_dpp(0, __float_as_int(v), CTRL, 0xf, 0xf, true));
}
__device__ __forceinline__ float add32(float a, float b) {
  auto r = __builtin_amdgcn_permlane32_swap(__float_as_uint(a), __float_as_uint(b), false, false);
  return __uint_as_float(r[0]) + __uint_as_float(r[1]);
}
__device__ __forceinline__ float add16(float a, float b) {
  auto r = __builtin_amdgcn_permlane16_swap(__float_as_uint(a), __float_as_uint(b), false, false);
  return __uint_as_float(r[0]) + __uint_as_float(r[1]);
}
__device__ __forceinline__ float sum64(float v) {
  v += dpp<0xB1>(v); v += dpp<0x4E>(v); v += dpp<0x141>(v); v += dpp<0x140>(v);
  v = add16(v, v);
  return add32(v, v);
}
__device__ __forceinline__ u32x4 swap16(u32x2 a, u32x2 b) {
  auto r0 = __builtin_amdgcn_permlane16_swap(a[0], b[0], false, false);
  auto r1 = __builtin_amdgcn_permlane16_swap(a[1], b[1], false, false);
  return (u32x4){r0[0], r1[0], r0[1], r1[1]};
}
__device__ __forceinline__ f32x4 mfma16(s16x8 a, s16x8 b, f32x4 c) {
  return __builtin_amdgcn_mfma_f32_16x16x32_bf16(__builtin_bit_cast(bf16x8, a), __builtin_bit_cast(bf16x8, b), c, 0, 0, 0);
}
#define TNT " nt"
__device__ __forceinline__ void mfma_acc(f32x4& c, s16x8 a, s16x8 b) { asm volatile("v_mfma_f32_16x16x32_bf16 %0, %1, %2, %0" : "+v"(c) : "v"(a), "v"(b)); }
#define MFMA_DONE() do { asm volatile("s_nop 7\n\ts_nop 7"); __builtin_amdgcn_sched_barrier(0); } while (0)
__device__ __forceinline__ s16x4 tr_read(LAS char* p) { return __builtin_amdgcn_ds_read_tr16_b64_v4i16((LAS s16x4*)p); }
__device__ __forceinline__ s16x8 cat8(s16x4 a, s16x4 b) { return __builtin_shufflevector(a, b, 0, 1, 2, 3, 4, 5, 6, 7); }
template <int OFF, int DOFF = 0> __device__ __forceinline__ void dma16(const void* base, unsigned voff, unsigned dst) {
  asm volatile("s_add_u32 m0, %0, %4\n\ts_nop 0\n\tglobal_load_lds_dwordx4 %1, %2 offset:%3" ::"s"(dst), "v"(voff), "s"(base), "i"(OFF), "i"(DOFF - OFF) : "scc");
}
template <int OFF, int DOFF> __device__ __forceinline__ void dma16_s(const void* base, unsigned voff, unsigned dst) {
  asm volatile("s_add_u32 m0, %0, %4\n\ts_nop 0\n\tglobal_load_lds_dwordx4 %1, %2 offset:%3" TNT ::"s"(dst), "v"(voff), "s"(base), "i"(OFF), "i"(DOFF - OFF) : "scc");
}
template <int DOFF, int NL> __device__ __forceinline__ void dma16_m(const void* base, unsigned voff, unsigned dst) {
  asm volatile("s_add_u32 m0, %0, %4\n\ts_bfm_b64 exec, %3, 0\n\tglobal_load_lds_dwordx4 %1, %2\n\ts_mov_b64 exec, -1"
               ::"s"(dst), "v"(voff), "s"(base), "i"(NL), "i"(DOFF) : "scc");
}
template <int NL> __device__ __forceinline__ void touch(const void* base, unsigned v, unsigned dst) {
  unsigned t;
  asm volatile("v_lshlrev_b32 %0, 3, %3\n\ts_mov_b32 m0, %2\n\ts_bfm_b64 exec, %4, 0\n\tglobal_load_lds_dword %0, %1\n\ts_mov_b64 exec, -1"
               : "=&v"(t) : "s"(base), "s"(dst), "v"(v), "i"(NL));
}
template <int L0> __device__ __forceinline__ void touch_hi(const void* base, unsigned v, unsigned dst) {
  asm volatile("s_mov_b32 m0, %1\n\ts_bfm_b64 exec, 4, %3\n\tglobal_load_lds_dword %2, %0\n\ts_mov_b64 exec, -1" :: "s"(base), "s"(dst), "v"(v), "i"(L0));
}
#define KNT " nt"
#define VNT " nt"
#define LD_HID(d, voff, base, OFF, MOD) asm volatile("global_load_dwordx4 %0, %1, %2 offset:%3" MOD " ; HID %0" : "=&v"(d) : "v"(voff), "s"(base), "i"(OFF))
#define LD_HIDB(d, voff, r, MOD) asm volatile("buffer_load_dwordx4 %0, %1, %2, 0 offen" MOD " ; HID %0" : "=&v"(d) : "v"(voff), "s"(r))
#define LANDED(x) asm volatile("; USE %0" : "+v"(x))
template <int H> __device__ __forceinline__ void colsum(float& cs, s16x8 w, int idx) {
  const u32x4 v = __builtin_bit_cast(u32x4, w);
  unsigned a;
  if constexpr (H == 0)
    asm volatile("s_set_gpr_idx_on %2, gpr_idx(SRC1)\n\tv_lshlrev_b32 %1, 16, %3 ; CSCHK %3 %4\n\ts_set_gpr_idx_off\n\tv_add_f32 %0, %0, %1"
                 : "+v"(cs), "=&v"(a) : "s"(idx), "v"(v[0]), "v"(v));
  else
    asm volatile("s_set_gpr_idx_on %2, gpr_idx(SRC1)\n\tv_and_b32 %1, 0xffff0000, %3 ; CSCHK %3 %4\n\ts_set_gpr_idx_off\n\tv_add_f32 %0, %0, %1"
                 : "+v"(cs), "=&v"(a) : "s"(idx), "v"(v[0]), "v"(v));
}
#define OPAQUE(x) asm volatile("" : "+v"(x))
#define WAIT_V(n) asm volatile("s_waitcnt vmcnt(%0)" ::"i"(n) : "memory")
#define WAIT_L0() __builtin_amdgcn_s_waitcnt(0xC07F)
#define WAIT_V0C() __builtin_amdgcn_s_waitcnt(0x0F70)
#define WAIT_VL(n) do { WAIT_L0(); WAIT_V(n); } while (0)
#define FENCE_C() asm volatile("" ::: "memory")
#define BAR() do { FENCE_C(); __builtin_amdgcn_s_barrier(); FENCE_C(); } while (0)
#define SCHED0() __builtin_amdgcn_sched_barrier(0)
#define RLX __ATOMIC_RELAXED, __HIP_MEMORY_SCOPE_AGENT
#define ST_SC1(v, off) __builtin_amdgcn_raw_buffer_store_b128(v, rs, off, 0, 16)
#define LD_SC1(off) __builtin_amdgcn_raw_buffer_load_b128(rs, off, 0, 16)

#define STAMP(i)
#define STAMP_OUT()
#define STEP_T(idx)
template <int V> struct IC { static constexpr int value = V; };
template <int N, int I = 0, class Fn> __device__ __forceinline__ void static_for(Fn&& f) {
  if constexpr (I < N) { f(IC<I>{}); static_for<N, I + 1>(f); }
}

__device__ __forceinline__ void wait_flags(GAS unsigned* p, unsigned v, unsigned tag) {
  if (__builtin_expect(!__all(v == tag), 0)) {
    for (unsigned spins = 0; spins < SPIN_CAP; ++spins) {
      __builtin_amdgcn_s_sleep(2);
      if (__all(__hip_atomic_load(p, RLX) == tag)) break;
    }
  }
  FENCE_C();
}

constexpr int U_GS = 960;
constexpr int U_UP = 8 * U_GS + 16;
constexpr int U_WS = U_UP + 8 * U_GS;
constexpr int L_A = 61568;
constexpr int L_ST = L_A + 3 * 32768;
constexpr int L_JUNK = L_ST + 2048;
constexpr int L_SK = L_JUNK + 256;
constexpr int L_CNT = L_SK + 1024;
constexpr int L_END = L_CNT + 16;
constexpr int L_ALLOC = L_END;
constexpr int abuf(int u) { return L_A + (u % 3 == 0 ? 0 : u % 3 == 1 ? 65536 : 32768); }
#define DM0 7
constexpr int CODE_BYTES = 57 * 1024;
#define TW 8
#define XT 99
#define XP 0
#define W2T 11
#define SX0 0
#define SKA 3
#define SCL 24
#define UAUX 16
constexpr int S_X0 = SX0, S_X1 = S_X0 + 1, S_ST = S_X1 + 4, S_KA = SKA, S_KB = S_KA + 2, S_KV = S_KB + 4, S_V = S_KV, S_FL = S_V + 10, S_CL = SCL;
static_assert(S_ST + 4 <= S_KV || S_ST >= S_V + 7, "side jobs share registers");
constexpr bool w2t(int s) { return s >= W2T && !((s - W2T) & 1) && s < W2T + 14; }
constexpr bool wt(int s) { return s >= 0 && (s & ~1) + TW < 32; }
constexpr bool xt(int s) { return s >= 0 && !(s & 1) && s / 2 + XT < 16; }
constexpr int extra(int s) {
  return (3 + XP) * (s == -1) + wt(s) + xt(s) + w2t(s) + 2 * (s == 0) + (s == S_X0) + (s == S_X1) + 3 * (s == S_KA) + 2 * (s == S_KB) + (s == S_ST + 3) +
         (s == S_V || s == S_V + 1) + (s == S_CL || s == S_CL + 1) + (s == S_V + 5 || s == S_V + 6) + (s == S_FL);
}
constexpr int L_CSP = 0, L_CSF = 4096;
constexpr int C_US = 256 + 4096, L_U = 98304;
constexpr int L_Z = L_U + 4 * C_US;
constexpr int C_TS = 32 + 4096, L_T = 143232;
constexpr int CSPLIT = 22;
static_assert(L_Z + 16384 <= L_T && L_T >= abuf(13) && L_T + 4 * C_TS <= L_ST && L_U >= abuf(14), "lds map");
constexpr int D_SLOT = 8192, N1 = 48, N2 = 16, NT = N1 + N2;
constexpr int DRAIN_A = 6, DRAIN_B = 16;

template <int LO, int HI, int FIRST, class Hook>
__device__ __forceinline__ void conv_part(LAS char* tp, LAS char* gp, LAS char* zp, bool last, Hook&& hook) {
  constexpr int NH = HI / 2 + 1;
  s16x8 Fh[NH];
#pragma unroll
  for (int n = 0; n < NH; ++n) Fh[n] = cat8(tr_read(tp + n * 256), tr_read(tp + n * 256 - 32));
  f32x4 z[HI - LO + 1];
#pragma unroll
  for (int i = 0; i <= HI - LO; ++i) z[i] = (f32x4){0.f, 0.f, 0.f, 0.f};
  float p1 = 0.f, p2 = 0.f, p3 = 0.f, yp = 0.f;
  static_for<HI + 1>([&](auto P_) {
    constexpr int p = P_.value;
    hook(P_);
    const s16x8 G = cat8(tr_read(gp + p * 128), tr_read(gp + p * 128 + 32));
#pragma unroll
    for (int n = 0; p + 2 * n <= HI; ++n)
      if (p + 2 * n >= LO) z[p + 2 * n - LO] = mfma16(G, Fh[n], z[p + 2 * n - LO]);
    if constexpr (p >= LO) {
      const f32x4 t = z[p - LO];
      const float s3 = t[3];
      const float s2 = t[2] + dpp<0x13C>(last ? p3 : s3);
      const float s1 = t[1] + dpp<0x13C>(last ? p2 : s2);
      const float y = t[0] + dpp<0x13C>(last ? p1 : s1);
      p1 = s1; p2 = s2; p3 = s3;
      if constexpr (p >= FIRST && ((p - FIRST) & 1)) {
        const unsigned o = pk_bf16(yp, y);
        *(LAS u16*)(zp + (p - 1) * 512) = (u16)o;
        *(LAS u16*)(zp + p * 512) = (u16)(o >> 16);
      }
      yp = y;
    }
  });
}

__global__ __launch_bounds__(512, 2) void k_all(const u16* __restrict__ x, const u16* __restrict__ kk, const u16* __restrict__ vv,
                                                const u16* __restrict__ shk, const u16* __restrict__ ssmk, const u16* __restrict__ ssmd,
                                                const u16* __restrict__ w1, const u16* __restrict__ w3, const u16* __restrict__ w2,
                                                u16* __restrict__ out, char* __restrict__ ws) {
  extern __shared__ __attribute__((aligned(16))) char smem[];
  LAS char* sm = (LAS char*)smem;
  int tid = threadIdx.x, lane = tid & 63;
  const int wid = __builtin_amdgcn_readfirstlane(tid >> 6);
  const int bid = blockIdx.x, xcd = bid & 7, rr = bid >> 3;
  const int mb = (xcd & 1) * 4 + (rr & 3), nb = (xcd >> 1) * 8 + (rr >> 2);
  const int wm = wid & 3, wn = wid >> 2;
  int g = lane >> 4, fr = lane & 15, qq = fr >> 2, pp = lane & 3;
  const __amdgpu_buffer_rsrc_t rs = __builtin_amdgcn_make_buffer_rsrc(ws, 0, WS_END, 0x00020000);
  const unsigned lds0 = (unsigned)(size_t)sm;
  STAMP(0);

  const char* gA[4];
#pragma unroll
  for (int i = 0; i < 4; ++i) gA[i] = (const char*)x + (size_t)(mb * 256 + i * 64 + wid * 8) * (D_ * 2);
  const char* gG = (const char*)w1 + ((size_t)(wid * 4) * F_ + nb * 112) * 2;
  const char* gU = (const char*)w3 + ((size_t)(wid * 4) * F_ + nb * 112) * 2;
  const unsigned vA = (lane >> 3) * (D_ * 2) + (((lane & 7) ^ ((wid & 1) * 4 + g)) * 16);
  const int tq = min(wid * 4 + (lane & 3), 27);
  const unsigned vB = lane < 56 ? (lane / 14) * (F_ * 2) + (lane % 14) * 16 : lane < 60 ? (tq / 14) * (F_ * 2) + (tq % 14) * 128 : (wid * 4 + (lane & 3)) * 2048;
  const unsigned wto = (TW * 32 + rr * 2) * (F_ * 2) + (xcd >> 1) * 1792;
  const char* xto = (const char*)x + ((xcd & 1) * 1024 + rr * 32) * (D_ * 2);
  const unsigned lA = lds0 + wid * 1024, lB = lds0 + wid * U_GS;
#define A_DMA(q, i) dma16<((q) >> 1) * 128, abuf((q) >> 1) + (((q) & 1) * 2 + (i)) * 8192>(gA[((q) & 1) * 2 + (i)], vA, lA)
#define W_DMA(slot, i, NL) do { \
    if (i == 0) { dma16_m<(slot) * U_WS, NL>(gG, vB, lB); gG += 32 * F_ * 2; } \
    else { dma16_m<(slot) * U_WS + U_UP, NL>(gU, vB, lB); gU += 32 * F_ * 2; } } while (0)
#define G1_DMA() do { A_DMA(0, 0); A_DMA(0, 1); A_DMA(1, 0); A_DMA(1, 1); W_DMA(0, 0, 56); W_DMA(0, 1, 56); W_DMA(1, 0, 56); W_DMA(1, 1, 56); } while (0)
#define G2_DMA() do { A_DMA(2, 0); A_DMA(2, 1); A_DMA(3, 0); A_DMA(3, 1); W_DMA(2, 0, 56); W_DMA(2, 1, 56); A_DMA(4, 0); A_DMA(4, 1); W_DMA(3, 0, 56); W_DMA(3, 1, 56); } while (0)
  const int tb = bid >> 4, cb = bid & 15;
  f32x4 acc[4][7];
  G1_DMA();
#pragma unroll
  for (int i = 0; i < 28; ++i) { acc[i & 3][i >> 2] = (f32x4){0.f, 0.f, 0.f, 0.f}; OPAQUE(acc[i & 3][i >> 2]); }
  if (tid == 0) *(LAS unsigned*)(sm + L_CNT) = 0u;
  unsigned ra[2][2], rw[2];
  ra[0][0] = lds0 + L_A + (wm * 64 + fr) * 128 + ((g ^ (fr >> 1)) * 16);
  ra[0][1] = ra[0][0] ^ 64;
  ra[1][0] = ra[0][0] + 65536; ra[1][1] = ra[0][1] + 65536;
  rw[0] = lds0 + 2 * g * U_GS + qq * 224 + wn * 112 + pp * 8;
  rw[1] = lds0 + 2 * g * U_GS + qq * 224 + wn * 112 + 96 + (pp & 1) * 8 + (pp >> 1) * U_UP;
  OPAQUE(ra[0][0]); OPAQUE(ra[0][1]); OPAQUE(ra[1][0]); OPAQUE(ra[1][1]); OPAQUE(rw[0]); OPAQUE(rw[1]);
  SCHED0();
  G2_DMA();
  u32x4 d[2], kr[5], vr[2];
  unsigned uo[2][4];
  const char* xrow = (const char*)x + (bid * 8 + wid) * (D_ * 2);
  {
    const char* pc;
    asm volatile("s_getpc_b64 %0" : "=s"(pc));
    touch<2>(pc + min(rr * 8 + wid, CODE_BYTES / 256 - 1) * 256, lane * 16, lds0 + L_JUNK);
  }
  touch_hi<56>((const char*)w1 + wto - 64 * (F_ * 2), vB, lds0 + L_JUNK);
  touch_hi<56>((const char*)w3 + wto - 64 * (F_ * 2), vB, lds0 + L_JUNK);
  if constexpr (XP >= 2) touch_hi<60>(xto + (XT - 2) * 128, vB, lds0 + L_JUNK);
  if constexpr (XP >= 1) touch_hi<60>(xto + (XT - 1) * 128, vB, lds0 + L_JUNK);
#define U_RD(i, X, Wl, Wh, t) do { \
    if constexpr (i >= 2 && i < 6) X[i - 2] = *(LAS s16x8*)(size_t)(ra[((t) >> 1) % 3 == 1][(t) & 1] + (((t) >> 1) % 3 == 2 ? 32768 : 0) + (i - 2) * 2048); \
    else { constexpr int j_ = i < 2 ? i : i - 4, c_ = j_ >> 1; \
      LAS char* p_ = (LAS char*)(size_t)(rw[c_ == 6] + ((t) & 3) * U_WS + (c_ == 6 ? 0 : (c_ >> 1) * 32 + (c_ & 1) * U_UP) + (j_ & 1) * U_GS); \
      if constexpr (j_ & 1) Wh[c_] = tr_read(p_); else Wl[c_] = tr_read(p_); } } while (0)
  float cs[7] = {0.f, 0.f, 0.f, 0.f, 0.f, 0.f, 0.f};
  s16x8 X[2][4];
  s16x4 Wl[2][7], Wh[2][7];
  WAIT_V(12 + extra(-1)); BAR();
  STAMP(1);
  static_for<18>([&](auto I_) { constexpr int i = I_.value; U_RD(i, X[0], Wl[0], Wh[0], 0); });
  SCHED0(); WAIT_VL(10 + extra(-1)); BAR(); SCHED0();

  const int cch = wid & 3;
  unsigned tag = 0, fu = 0, dsk = 0, ep;
  u32x2 skr;
  u32x4 sg;
#define U_DMA(s, i) do { \
    if constexpr ((s) < 28 && (i) < 2) W_DMA((s) & 3, (i), 56); \
    if constexpr ((s) < 27 && (i) >= 2) A_DMA((s) + 5, (i) - 2); \
    if constexpr ((s) == 27 && (i) >= 2) { OPAQUE(tid); const unsigned l_ = tid & 63; \
      dma16_s<((i) - 2) * 1024, ((i) - 2) * 1024 + L_T + 32>((const char*)ssmk + (size_t)(bid * 4 + cch) * 4096 + wn * 2048, l_ * 16, lds0 + cch * C_TS + wn * 2048); } } while (0)
  float ax, ay, bx, by, px, py, qx, qy, mean;
  f32x4 tx, ty;
#define PIN(a, b) do { OPAQUE(a); OPAQUE(b); } while (0)
#define UP2(a, b, v) do { a = bflo(v); b = bfhi(v); PIN(a, b); } while (0)
  auto side = [&](auto S_c, auto I_) { constexpr int s = S_c.value, i = I_.value;
    constexpr bool slot = (i & 3) == 0 || (i & 3) == 3;
    if constexpr (s == 4) {
      if constexpr (i == 0) { LANDED(ep); LANDED(skr); tag = __builtin_amdgcn_readfirstlane(ep) + 0x9E3779B9u; }
      if constexpr (i == 3) UP2(tx[0], tx[1], skr[0]);
      if constexpr (i == 4) UP2(tx[2], tx[3], skr[1]);
      if constexpr (i == 8) { OPAQUE(tid); *(LAS f32x4*)(sm + L_SK + (tid & 63) * 16) = tx; }
    }
    if constexpr (slot && s >= S_ST && s < S_ST + 4) {
      constexpr int n = (s - S_ST) * 14 + (i >> 1), r = n < 22 ? n - 16 : n - 46;
      if constexpr (n == 0) { LANDED(d[0]); LANDED(d[1]); }
      if constexpr (n == 22) PIN(d[0], d[1]);
      if constexpr (n < 16) {
        if constexpr (!(n & 1)) UP2(ax, ay, d[n >> 3][(n >> 1) & 3]);
        else { if constexpr (n == 1) { px = ax; py = ay; } else { px += ax; py += ay; } PIN(px, py); }
      }
      if constexpr (n >= 22 && n < 46) {
        constexpr int j = (n - 22) / 3, k = (n - 22) % 3;
        if constexpr (k == 0) UP2(ax, ay, d[j >> 2][j & 3]);
        if constexpr (k == 1) { ax -= mean; ay -= mean; PIN(ax, ay); }
        if constexpr (k == 2) { px = j ? fmaf(ax, ax, px) : ax * ax; py = j ? fmaf(ay, ay, py) : ay * ay; PIN(px, py); }
      }
      if constexpr (r == 0) { px += py; px += dpp<0xB1>(px); OPAQUE(px); }
      if constexpr (r == 1) { px += dpp<0x4E>(px); OPAQUE(px); }
      if constexpr (r == 2) { px += dpp<0x141>(px); OPAQUE(px); }
      if constexpr (r == 3) { px += dpp<0x140>(px); OPAQUE(px); }
      if constexpr (r == 4) { px = add16(px, px); OPAQUE(px); }
      if constexpr (r == 5) { px = add32(px, px) * (1.0f / D_); OPAQUE(px); if constexpr (n < 22) mean = px; }
      if constexpr (r == 6) { px = rbf(px) + 1e-5f; OPAQUE(px); }
      if constexpr (r == 7) { px = __builtin_amdgcn_sqrtf(rbf(px)); OPAQUE(px); }
      if constexpr (r == 8) { px = rbf(px); mean = rbf(mean); PIN(px, mean); }
      if constexpr (r == 9) {
        OPAQUE(tid);
        ST_SC1(((u32x4){__float_as_uint(mean), tag, __float_as_uint(px), tag}), WS_ST + (bid * 8 + wid) * 16 + (tid & 63) * 0x2000000);
      }
    }
    if constexpr (i == 27 && s >= S_KV - 1 && s < S_KV + 3) {
      OPAQUE(tid);
      tx = *(LAS f32x4*)(sm + L_SK + (tid & 7) * 128 + (s + 1 - S_KV) * 32);
      ty = *(LAS f32x4*)(sm + L_SK + (tid & 7) * 128 + (s + 1 - S_KV) * 32 + 16);
    }
    if constexpr (slot && s >= S_KV && s < S_KV + 4) {
      constexpr int e = s - S_KV, k = i >> 1;
      if constexpr (e == 0 && k == 0) { LANDED(kr[0]); LANDED(kr[1]); LANDED(kr[2]); LANDED(kr[3]); LANDED(kr[4]); }
      if constexpr (k == 0) UP2(ax, ay, kr[4][e]);
      if constexpr (k == 1) UP2(bx, by, kr[3][e]);
      if constexpr (k == 2) { qx = ax * tx[0]; qy = ay * ty[0]; PIN(qx, qy); }
      if constexpr (k == 3) { px = bx * tx[0]; py = by * ty[0]; PIN(px, py); }
      if constexpr (k == 4) UP2(ax, ay, kr[2][e]);
      if constexpr (k == 5) { qx = fmaf(bx, tx[1], qx); qy = fmaf(by, ty[1], qy); PIN(qx, qy); }
      if constexpr (k == 6) { px = fmaf(ax, tx[1], px); py = fmaf(ay, ty[1], py); PIN(px, py); }
      if constexpr (k == 7) UP2(bx, by, kr[1][e]);
      if constexpr (k == 8) { qx = fmaf(ax, tx[2], qx); qy = fmaf(ay, ty[2], qy); PIN(qx, qy); }
      if constexpr (k == 9) { px = fmaf(bx, tx[2], px); py = fmaf(by, ty[2], py); PIN(px, py); }
      if constexpr (k == 10) UP2(ax, ay, kr[0][e]);
      if constexpr (k == 11) { qx = fmaf(bx, tx[3], qx); qy = fmaf(by, ty[3], qy); PIN(qx, qy); }
      if constexpr (k == 12) { px = fmaf(ax, tx[3], px); py = fmaf(ay, ty[3], py); PIN(px, py); }
      if constexpr (k == 13) { uo[0][e] = pk_bf16(px, py); uo[1][e] = pk_bf16(qx, qy); PIN(uo[0][e], uo[1][e]); }
    }
    if constexpr (slot && s >= S_V + 5 && s < S_V + 7) {
      constexpr int e = (s - S_V - 5) * 2 + (i >> 1) / 7, k = (i >> 1) % 7;
      if constexpr (e == 0 && k == 0) { LANDED(vr[0]); LANDED(vr[1]); }
      if constexpr (k == 0) UP2(ax, ay, uo[0][e]);
      if constexpr (k == 1) UP2(px, py, vr[0][e]);
      if constexpr (k == 2) UP2(bx, by, uo[1][e]);
      if constexpr (k == 3) UP2(qx, qy, vr[1][e]);
      if constexpr (k == 4) { px *= ax; py *= ay; PIN(px, py); }
      if constexpr (k == 5) { qx *= bx; qy *= by; PIN(qx, qy); }
      if constexpr (k == 6) { uo[0][e] = pk_bf16(px, py); uo[1][e] = pk_bf16(qx, qy); PIN(uo[0][e], uo[1][e]); }
      if constexpr (i == 27) {
        OPAQUE(tid);
        __builtin_amdgcn_raw_buffer_store_b128((u32x4){uo[0][e - 1], uo[0][e], uo[1][e - 1], uo[1][e]}, rs, (tid & 7) * (2 * S_ * 8) + (tid >> 3) * 16,
                                               WS_U + ((cb * 16 + (e >> 1)) * S_ + tb * 128) * 8, UAUX);
      }
    }
    if constexpr (i == 20) {
      if constexpr (wt(s)) touch_hi<56>((const char*)(s & 1 ? w3 : w1) + wto + (s & ~1) * 32 * (F_ * 2), vB, lds0 + L_JUNK);
      if constexpr (s == S_CL || s == S_CL + 1) { OPAQUE(tid); __builtin_amdgcn_raw_buffer_store_b128((u32x4){~0u, ~0u, ~0u, ~0u}, rs, tid * 16, WS_EX + bid * 16384 + (s - S_CL) * 8192, 16); }
      if constexpr (s == 0) {
        OPAQUE(tid);
        asm volatile("global_load_dwordx2 %0, %1, %2 ; HID %0" : "=&v"(skr) : "v"((tid & 63) * 8), "s"((const char*)shk + cb * 512));
        asm volatile("v_mov_b32 %0, 0\n\tglobal_load_dword %0, %0, %1 sc1 ; HID %0" : "=&v"(ep) : "s"(ws + WS_EP));
      }
      if constexpr (s == S_X0 || s == S_X1) { OPAQUE(tid); LD_HID(d[s - S_X0], (tid & 63) * 16, xrow, (s - S_X0) * 1024, ""); }
      if constexpr (s == S_KA || s == S_KB) {
        OPAQUE(tid);
        const __amdgpu_buffer_rsrc_t rk = __builtin_amdgcn_make_buffer_rsrc((void*)kk, 0, S_ * D_ * 2, 0x00020000);
        const unsigned o = (tid >> 3) * (D_ * 4) + (tid & 7) * 16 + (tb * 128 - 3) * (D_ * 2) + cb * 128;
        if constexpr (s == S_KA) { LD_HIDB(kr[4], o + 4 * D_ * 2, rk, KNT); LD_HIDB(kr[2], o + 2 * D_ * 2, rk, KNT); LD_HIDB(kr[0], o, rk, KNT); }
        else { LD_HIDB(kr[3], o + 3 * D_ * 2, rk, KNT); LD_HIDB(kr[1], o + D_ * 2, rk, KNT); }
      }
      if constexpr (s == S_V || s == S_V + 1) {
        OPAQUE(tid);
        LD_HID(vr[s - S_V], (tid >> 3) * (D_ * 4) + (tid & 7) * 16, (const char*)vv + tb * 128 * (D_ * 2) + cb * 128, (s - S_V) * (D_ * 2), VNT);
      }
      if constexpr (s == S_FL) { OPAQUE(tid); __builtin_amdgcn_raw_buffer_store_b32(tag, rs, WS_FU + (cb * 16 + tb) * 128 + min(tid, 1) * 0x2000000, 0, 16); }
    }
    if constexpr (i == 24) {
      if constexpr (xt(s)) touch_hi<60>(xto + (s / 2 + XT) * 128, vB, lds0 + L_JUNK);
      if constexpr (w2t(s)) touch_hi<60>((const char*)w2 + ((s - W2T) / 2 * 16 + (bid >> 4)) * (32 * D_ * 2) + (bid & 15) * 128, vB, lds0 + L_JUNK);
    }
  };
  static_for<31>([&](auto S_c) {
    constexpr int s = S_c.value, b = s & 1;
    STEP_T(s);
    if constexpr (s == 28) {
      OPAQUE(tid);
      sg = LD_SC1(WS_ST + (mb * 256 + (tid & 255)) * 16);
      fu = __hip_atomic_load((GAS unsigned*)(ws + WS_FU + ((bid >> 4) * 16 + (tid & 15)) * 128), RLX);
      dsk = __hip_atomic_load((GAS u16*)(ssmd + bid * 4 + (tid & 3)), RLX);
    }
    SCHED0();
    static_for<28>([&](auto I_) { constexpr int i = I_.value, c = i >> 2, m = i & 3;
      mfma_acc(acc[m][c], cat8(Wl[b][c], Wh[b][c]), X[b][m]);
      if constexpr (i < 18) U_RD(i, X[b ^ 1], Wl[b ^ 1], Wh[b ^ 1], s + 1);
      if constexpr ((i & 3) == 3 && i >= DM0 && i < DM0 + 16) U_DMA(s, (i - DM0) >> 2);
      side(S_c, I_);
      if constexpr (m == 1 || m == 2) colsum<m - 1>(cs[c], cat8(Wl[b][c], Wh[b][c]), wm);
      if constexpr (s == 30 && i >= 14) colsum<i & 1>(cs[(i - 14) >> 1], cat8(Wl[1][(i - 14) >> 1], Wh[1][(i - 14) >> 1]), wm);
      SCHED0(); });
    if constexpr (s < 30) {
      WAIT_VL((s == 28 ? 7 : s == 29 ? 5 : 8) + extra(s - 2) + extra(s - 1) + extra(s));
      BAR(); SCHED0();
    }
  });
  STEP_T(31);
  OPAQUE(tid); lane = tid & 63; g = lane >> 4; fr = lane & 15; qq = fr >> 2; pp = lane & 3;
  u32x4 ur[2];
  f32x4 cv[7];
  f32x2 st[4];
  {
    float r[8];
    SCHED0();
    static_for<28>([&](auto I_) { constexpr int i = I_.value, c = i >> 2, m = i & 3;
      mfma_acc(acc[m][c], cat8(Wl[1][c], Wh[1][c]), X[1][m]);
      if constexpr (i == 1) {
        *(LAS float*)(sm + L_CSP + wid * 512 + lane * 4) = add16(add32(cs[0], cs[1]), add32(cs[2], cs[3]));
        *(LAS float*)(sm + L_CSP + wid * 512 + 256 + lane * 4) = add16(add32(cs[4], cs[5]), add32(cs[6], 0.f));
      }
      if constexpr (i == 4) {
        for (unsigned spins = 0; !__all(sg[1] == tag && sg[3] == tag) && spins < SPIN_CAP; ++spins) {
          __builtin_amdgcn_s_sleep(2); FENCE_C();
          sg = LD_SC1(WS_ST + (mb * 256 + (tid & 255)) * 16);
        }
        *(LAS u32x2*)(sm + L_ST + (tid & 255) * 8) = (u32x2){sg[0], sg[2]};
        wait_flags((GAS unsigned*)(ws + WS_FU + ((bid >> 4) * 16 + fr) * 128), fu, tag);
#pragma unroll
        for (int k = 0; k < 2; ++k) ur[k] = __builtin_amdgcn_raw_buffer_load_b128(rs, WS_U + bid * 16384 + tid * 32 + k * 16, 0, UAUX);
      }
      if constexpr (i == 10) {
        WAIT_L0(); BAR();
#pragma unroll
        for (int k = 0; k < 8; ++k) r[k] = *(LAS float*)(sm + L_CSP + wn * 2048 + k * 256 + lane * 4);
#pragma unroll
        for (int k = 0; k < 4; ++k) st[k] = *(LAS f32x2*)(sm + L_ST + (wm * 64 + k * 16 + fr) * 8);
      }
      if constexpr (i == 16) {
        LAS char* q = sm + L_CSF + wid * 512;
        *(LAS float*)(q + (((g & 1) << 1) | (g >> 1)) * 64 + fr * 4) = (r[0] + r[2]) + (r[4] + r[6]);
        *(LAS float*)(q + (((g & 1) << 1) | (g >> 1)) * 64 + 256 + fr * 4) = (r[1] + r[3]) + (r[5] + r[7]);
#pragma unroll
        for (int c2 = 0; c2 < 7; ++c2) cv[c2] = *(LAS f32x4*)(q + c2 * 64 + g * 16);
      }
      if constexpr (i == 20) {
        if (tid < 64) *(LAS u32x4*)(sm + L_U + g * C_US + fr * 16) = (u32x4){0u, 0u, 0u, 0u};
        else if (tid < 72) *(LAS u32x4*)(sm + L_T + (tid & 3) * C_TS + (tid & 4) * 4) = (u32x4){0u, 0u, 0u, 0u};
        else if (tid >= 128 && tid < 132) {
          LAS u16* p = (LAS u16*)(sm + L_T + (tid & 3) * C_TS + 32);
          *p = (u16)pk_bf16(bflo(*p) + bflo(dsk), 0.f);
        }
      }
      SCHED0(); });
  }
  MFMA_DONE();
  STEP_T(32);
  STAMP(2);

  {
    float ea[4], s2[4];
#pragma unroll
    for (int m = 0; m < 4; ++m) { ea[m] = -1.44269504089f * __builtin_amdgcn_rcpf(st[m][1]); s2[m] = st[m][1] * st[m][1]; }
    auto sw = [](f32x2 a, f32x2 b, float e, float q) {
      const f32x2 t = a * e;
      const f32x2 d = (f32x2){__builtin_amdgcn_exp2f(t[0]), __builtin_amdgcn_exp2f(t[1])} * q + q;
      const f32x2 h = a * b * (f32x2){__builtin_amdgcn_rcpf(d[0]), __builtin_amdgcn_rcpf(d[1])};
      return pk_bf16(h[0], h[1]);
    };
    const unsigned hb = WS_H + ((nb * 14 + wn * 7) * S_ + mb * 256 + wm * 64) * 16;
    const unsigned ho = ((g >> 1) * S_ + (g & 1) * 16 + fr) * 16;
#pragma unroll
    for (int P = 0; P < 3; ++P)
#pragma unroll
      for (int mp = 0; mp < 2; ++mp) {
        u32x2 h[2];
#pragma unroll
        for (int i = 0; i < 2; ++i) {
          const int m = 2 * mp + i;
          const f32x4 ga = acc[m][2 * P] - st[m][0] * cv[2 * P], up = acc[m][2 * P + 1] - st[m][0] * cv[2 * P + 1];
          h[i] = (u32x2){sw(ga.xy, up.xy, ea[m], s2[m]), sw(ga.zw, up.zw, ea[m], s2[m])};
        }
        ST_SC1(swap16(h[0], h[1]), hb + ho + (2 * P * S_ + mp * 32) * 16);
        SCHED0();
      }
    u32x2 t6[2];
#pragma unroll
    for (int mp = 0; mp < 2; ++mp) {
      const float e = lane < 32 ? ea[2 * mp] : ea[2 * mp + 1], q = lane < 32 ? s2[2 * mp] : s2[2 * mp + 1];
      f32x4 ga = acc[2 * mp][6] - st[2 * mp][0] * cv[6], up = acc[2 * mp + 1][6] - st[2 * mp + 1][0] * cv[6];
#pragma unroll
      for (int j = 0; j < 4; ++j) {
        auto r = __builtin_amdgcn_permlane32_swap(__float_as_uint(ga[j]), __float_as_uint(up[j]), false, false);
        ga[j] = __uint_as_float(r[0]); up[j] = __uint_as_float(r[1]);
      }
      t6[mp] = (u32x2){sw(ga.xy, up.xy, e, q), sw(ga.zw, up.zw, e, q)};
    }
    ST_SC1(swap16(t6[0], t6[1]), hb + (6 * S_ + ((g & 1) * 2 + (g >> 1)) * 16 + fr) * 16);
  }
#pragma unroll
  for (int c = 0; c < 4; ++c) {
    const unsigned sel = (c & 1) ? 0x07060302u : 0x05040100u;
    *(LAS u32x2*)(sm + L_U + c * C_US + 256 + tid * 8) =
        (u32x2){__builtin_amdgcn_perm(ur[0][2 + (c >> 1)], ur[0][c >> 1], sel), __builtin_amdgcn_perm(ur[1][2 + (c >> 1)], ur[1][c >> 1], sel)};
  }
  WAIT_L0(); BAR();
  STAMP(3);

  LAS char* tp = sm + L_T + cch * C_TS + 32 + (-12 + 4 * pp + 4 * (31 - 8 * g - qq)) * 2;
  LAS char* gp = sm + L_U + cch * C_US + 256 + (16 * pp - 112 + 4 * (8 * g + qq)) * 2;
  LAS char* zp = sm + L_Z + lane * 8 + cch * 2;
  auto pub_h = [&]() {
    WAIT_V(0);
    if (lane == 0) {
      if (__hip_atomic_fetch_add((LAS unsigned*)(sm + L_CNT), 1u, __ATOMIC_RELAXED, __HIP_MEMORY_SCOPE_WORKGROUP) == 7u)
        __hip_atomic_store((GAS unsigned*)(ws + WS_FH + (mb * 32 + nb) * 128), tag, RLX);
    }
  };
  const int mb2 = mb * 2 + ((rr >> 2) & 1);
  const int kh = xcd >> 2, nb2 = ((xcd >> 1) & 1) * 4 + (rr >> 3), pbid = bid ^ 4;

  if (wid < 4) {
    const char* wB = (const char*)w2 + ((size_t)(wid * 8) * D_ + nb2 * 128) * 2;
    const char* gB = wB + (size_t)(kh * NT) * (32 * D_ * 2);
    const unsigned v2 = g * (D_ * 2) + ((fr ^ ((g | ((wid & 1) << 2)) << 1)) * 16);
    const unsigned dB = lds0 + wid * 2048;
#define D_STAGE(d, o) do { dma16<0, (o)>(gB, v2, d); dma16<0, (o) + 1024>(gB + 4 * D_ * 2, v2, d); gB += 32 * D_ * 2; } while (0)
#define D_GROUP(j) do { const unsigned d_ = dB + ((j) & 3) * (4 * D_SLOT); \
      D_STAGE(d_, 0); D_STAGE(d_, D_SLOT); D_STAGE(d_, 2 * D_SLOT); D_STAGE(d_, 3 * D_SLOT); } while (0)
    conv_part<0, CSPLIT - 1, 0>(tp, gp, zp, lane == 63, [&](auto P_) { constexpr int p = P_.value;
      if constexpr (p == DRAIN_A) pub_h();
      if constexpr (p > DRAIN_A && p <= DRAIN_A + 12) D_STAGE(dB, (p - DRAIN_A - 1) * D_SLOT); });
    STAMP(4);
    WAIT_VL(16); BAR();
    STAMP(5);
    {
      u32x4 o[4];
#pragma unroll
      for (int i = 0; i < 4; ++i) o[i] = *(LAS u32x4*)(sm + L_Z + i * 4096 + tid * 16);
#pragma unroll
      for (int i = 0; i < 4; ++i) ST_SC1(o[i], WS_Z + bid * 16384 + i * 4096 + tid * 16);
    }
    WAIT_VL(12); BAR();
    D_GROUP(3); D_GROUP(4);
    WAIT_V(20); BAR();
    D_GROUP(5);
    for (int j = 3; j < NT / 4 - 3; ++j) {
      WAIT_V(16); BAR();
      if (j == 3 && tid == 0) __hip_atomic_store((GAS unsigned*)(ws + WS_FZ + bid * 128), tag, RLX);
      if (j + 3 == N1 / 4) gB = wB + (size_t)N1 * (32 * D_ * 2);
      D_GROUP(j + 3);
    }
    WAIT_V(16); BAR();
    WAIT_V(8); BAR();
    WAIT_V(0); BAR();
    if (bid == 0 && wid == 3) {
#pragma unroll
      for (int i = 0; i < 4; ++i) { GAS unsigned* fp = (GAS unsigned*)(ws + WS_FU + (i * 64 + lane) * 128); wait_flags(fp, __hip_atomic_load(fp, RLX), tag); }
      if (lane == 0) __hip_atomic_store((GAS unsigned*)(ws + WS_EP), tag, RLX);
    }
    STAMP_OUT();
    return;
  }

  __builtin_amdgcn_s_setprio(1);
  conv_part<CSPLIT - 1, 31, CSPLIT>(tp, gp, zp, lane == 63, [&](auto P_) { if constexpr (P_.value == DRAIN_B) pub_h(); });
  STAMP(4);
  const int row0 = mb2 * 128 + kh * 64 + cch * 16 + fr;
  u32x4 xv[4];
#pragma unroll
  for (int P = 0; P < 4; ++P) xv[P] = *(const u32x4*)(x + (size_t)row0 * D_ + nb2 * 128 + (2 * P + (g & 1)) * 16 + (g >> 1) * 8);
  {
    GAS unsigned* fp = (GAS unsigned*)(ws + (lane < 19 ? WS_FH + (mb * 32 + kh * 13 + lane) * 128 : WS_FH + (mb * 32 + (nb ^ 16)) * 128));
    wait_flags(fp, __hip_atomic_load(fp, RLX), tag);
  }
  STAMP(5);
  const unsigned vK = WS_H + (g * S_ + row0) * 16, vS = vK ^ 1024;
  unsigned ko = kh * NT * (4 * S_ * 16);
  u32x4 Hf[8][2];
#define H_LD(t, m) Hf[(t) & 7][m] = __builtin_amdgcn_raw_buffer_load_b128(rs, (m) ? vS : vK, ko, 16)
  static_for<8>([&](auto I_) { H_LD(I_.value, 0); H_LD(I_.value, 1); ko += 4 * S_ * 16; });
  unsigned rb[2][8];
#pragma unroll
  for (int c = 0; c < 8; ++c) {
    rb[0][c] = lds0 + (8 * g + qq) * 256 + ((c ^ (qq | ((g & 1) << 2))) * 32) + pp * 8;
    rb[1][c] = rb[0][c] + 8 * D_SLOT; OPAQUE(rb[0][c]); OPAQUE(rb[1][c]);
  }
  s16x4 Vl[2][8], Vh[2][8];
#define D_RD(i, b, slot) do { LAS char* p_ = (LAS char*)(size_t)(rb[((slot) >> 3) & 1][(i) >> 1] + ((slot) & 7) * D_SLOT + ((i) & 1) * 1024); \
    if constexpr ((i) & 1) Vh[b][(i) >> 1] = tr_read(p_); else Vl[b][(i) >> 1] = tr_read(p_); } while (0)
#define D_STEP(s, NM, LD, RD) do { constexpr int b_ = (s) & 1; \
    if constexpr (((s) & 3) == 3 && RD) { WAIT_L0(); BAR(); } \
    SCHED0(); \
    static_for<8 * NM>([&](auto I_) { constexpr int i = I_.value, c = i & 7, m = i >> 3; \
      mfma_acc(ac[m][c], cat8(Vl[b_][c], Vh[b_][c]), __builtin_bit_cast(s16x8, Hf[(s) & 7][m])); \
      if constexpr (RD) { if constexpr (NM == 2) D_RD(i, b_ ^ 1, (s) + 1); else { D_RD(2 * i, b_ ^ 1, (s) + 1); D_RD(2 * i + 1, b_ ^ 1, (s) + 1); } } \
      if constexpr (c == 7 && m < LD) H_LD(s, m); \
      SCHED0(); }); \
    ko += 4 * S_ * 16; } while (0)
  f32x4 ac[2][8];
#pragma unroll
  for (int i = 0; i < 16; ++i) { ac[i >> 3][i & 7] = (f32x4){0.f, 0.f, 0.f, 0.f}; OPAQUE(ac[i >> 3][i & 7]); }
  GAS unsigned* fzp = (GAS unsigned*)(ws + WS_FZ + (nb2 * 32 + (lane & 31)) * 128);
  u32x2 zv[8];
  WAIT_L0(); BAR();
  STAMP(6);
  static_for<16>([&](auto I_) { D_RD(I_.value, 0, 0); });
  for (int it = 0; it < N1 / 16 - 1; ++it) static_for<16>([&](auto J_) { D_STEP(J_.value, 2, 2, true); });
  unsigned fz = __hip_atomic_load(fzp, RLX);
  static_for<16>([&](auto J_) { constexpr int s = J_.value;
    if constexpr (s == 8) {
      ko = N1 * (4 * S_ * 16);
      wait_flags(fzp, fz, tag);
#pragma unroll
      for (int c = 0; c < 8; ++c) zv[c] = __builtin_amdgcn_raw_buffer_load_b64(rs, WS_Z + ((nb2 * 32 + c * 4 + g) * S_ + row0) * 8, 0, 16);
    }
    D_STEP(s, 2, (s < 8 ? 2 : 1), true); });
  MFMA_DONE();
  STAMP(7);
#pragma unroll
  for (int P = 0; P < 4; ++P) {
    u32x4 o;
#pragma unroll
    for (int i = 0; i < 4; ++i) {
      const f32x4 a = ac[1][2 * P + (i >> 1)];
      o[i] = min(pk_bf16(a[(i & 1) * 2], a[(i & 1) * 2 + 1]), 0xfffffffeu);
    }
    ST_SC1(o, WS_EX + pbid * 16384 + P * 4096 + (tid & 255) * 16);
  }
  u32x4 pv[4];
  static_for<N2>([&](auto J_) { constexpr int s = J_.value;
    if constexpr (s == N2 - 3) {
#pragma unroll
      for (int i = 0; i < 4; ++i) pv[i] = LD_SC1(WS_EX + bid * 16384 + i * 4096 + (tid & 255) * 16);
    }
    D_STEP(s, 1, (s + 8 < N2 ? 1 : 0), (s + 1 < N2));
  });
  MFMA_DONE();
  STAMP(8);
  for (unsigned spins = 0; spins < SPIN_CAP; ++spins) {
    unsigned mx = 0;
#pragma unroll
    for (int i = 0; i < 16; ++i) mx = max(mx, pv[i >> 2][i & 3]);
    if (!__any(mx == ~0u)) break;
    __builtin_amdgcn_s_sleep(1); FENCE_C();
#pragma unroll
    for (int i = 0; i < 4; ++i) pv[i] = LD_SC1(WS_EX + bid * 16384 + i * 4096 + (tid & 255) * 16);
  }
  STAMP(9);
  {
    const f32x2 sv = *(LAS f32x2*)(sm + L_ST + (row0 & 255) * 8);
    const float ri = __builtin_amdgcn_rcpf(sv[1]), mo = -sv[0] * ri;
    const __amdgpu_buffer_rsrc_t ro = __builtin_amdgcn_make_buffer_rsrc(out, 0, S_ * D_ * 2, 0x00020000);
#pragma unroll
    for (int P = 0; P < 4; ++P) {
      const u32x4 xs = swap16((u32x2){xv[P][0], xv[P][1]}, (u32x2){xv[P][2], xv[P][3]});
      u32x2 o[2];
#pragma unroll
      for (int i = 0; i < 4; ++i) {
        const int c = 2 * P + (i >> 1), d = i & 1;
        const float x0 = bflo(xs[i]), x1 = bfhi(xs[i]);
        const float y0 = fmaf(fmaf(x0, ri, mo), bflo(zv[c][d]), x0) + (ac[0][c][2 * d] + bflo(pv[P][i]));
        const float y1 = fmaf(fmaf(x1, ri, mo), bfhi(zv[c][d]), x1) + (ac[0][c][2 * d + 1] + bfhi(pv[P][i]));
        o[i >> 1][d] = pk_bf16(y0, y1);
      }
      __builtin_amdgcn_raw_buffer_store_b128(swap16(o[0], o[1]), ro, (row0 * D_ + nb2 * 128 + (2 * P + (g & 1)) * 16 + (g >> 1) * 8) * 2, 0, 16);
    }
  }
  STAMP(10);
  STAMP(11);
  STAMP_OUT();
}

#define WS_OFF 0
extern "C" void kernel_launch(void* const* d_in, const int* in_sizes, int n_in, void* d_out, int out_size, void* d_ws, size_t ws_size,
                              hipStream_t stream) {
  (void)hipFuncSetAttribute((const void*)k_all, hipFuncAttributeMaxDynamicSharedMemorySize, L_ALLOC);
  k_all<<<256, 512, L_ALLOC, stream>>>((const u16*)d_in[0], (const u16*)d_in[1], (const u16*)d_in[2], (const u16*)d_in[3], (const u16*)d_in[4],
                                     (const u16*)d_in[5], (const u16*)d_in[7], (const u16*)d_in[8], (const u16*)d_in[9], (u16*)d_out, (char*)d_ws + WS_OFF);
}
```

```cpp
#include <hip/hip_runtime.h>
#include <hip/hip_bf16.h>

#define LAS __attribute__((address_space(3)))
#define GAS __attribute__((address_space(1)))
typedef unsigned short u16;
typedef short s16x4 __attribute__((ext_vector_type(4)));
typedef short s16x8 __attribute__((ext_vector_type(8)));
typedef __bf16 bf16x2 __attribute__((ext_vector_type(2)));
typedef __bf16 bf16x8 __attribute__((ext_vector_type(8)));
typedef float f32x2 __attribute__((ext_vector_type(2)));
typedef float f32x4 __attribute__((ext_vector_type(4)));
typedef unsigned u32x4 __attribute__((ext_vector_type(4)));
typedef unsigned u32x2 __attribute__((ext_vector_type(2)));

constexpr int S_ = 2048, D_ = 1024, F_ = 3584;

constexpr unsigned WS_EP = 0;
constexpr unsigned WS_FU = 4096;
constexpr unsigned WS_FH = WS_FU + 32768;
constexpr unsigned WS_FZ = WS_FH + 32768;
constexpr unsigned WS_ST = WS_FZ + 32768;
constexpr unsigned WS_U = 1u << 20;
constexpr unsigned WS_Z = WS_U + S_ * D_ * 2;
constexpr unsigned WS_EX = WS_Z + S_ * D_ * 2;
constexpr unsigned WS_H = WS_EX + 256 * 16384;
constexpr unsigned WS_END = WS_H + S_ * F_ * 2;
static_assert(WS_END <= 0x2000000, "offsets used to drop a store");
constexpr unsigned SPIN_CAP = 1u << 18;

__device__ __forceinline__ float bflo(unsigned v) { return __uint_as_float(v << 16); }
__device__ __forceinline__ float bfhi(unsigned v) { return __uint_as_float(v & 0xffff0000u); }
__device__ __forceinline__ unsigned pk_bf16(float lo, float hi) {
  f32x2 v = {lo, hi};
  return __builtin_bit_cast(unsigned, __builtin_convertvector(v, bf16x2));
}
__device__ __forceinline__ float rbf(float x) { return bflo(pk_bf16(x, x)); }
__device__ __forceinline__ float dot2(unsigned a, unsigned b, float c) {
  return __builtin_amdgcn_fdot2_f32_bf16(__builtin_bit_cast(bf16x2, a), __builtin_bit_cast(bf16x2, b), c, false);
}
template <int CTRL> __device__ __forceinline__ float dpp(float v) {
  return __int_as_float(__builtin_amdgcn_update_dpp(0, __float_as_int(v), CTRL, 0xf, 0xf, true));
}
__device__ __forceinline__ float add32(float a, float b) {
  auto r = __builtin_amdgcn_permlane32_swap(__float_as_uint(a), __float_as_uint(b), false, false);
  return __uint_as_float(r[0]) + __uint_as_float(r[1]);
}
__device__ __forceinline__ float add16(float a, float b) {
  auto r = __builtin_amdgcn_permlane16_swap(__float_as_uint(a), __float_as_uint(b), false, false);
  return __uint_as_float(r[0]) + __uint_as_float(r[1]);
}
__device__ __forceinline__ float sum64(float v) {
  v += dpp<0xB1>(v); v += dpp<0x4E>(v); v += dpp<0x141>(v); v += dpp<0x140>(v);
  v = add16(v, v);
  return add32(v, v);
}
__device__ __forceinline__ u32x4 swap16(u32x2 a, u32x2 b) {
  auto r0 = __builtin_amdgcn_permlane16_swap(a[0], b[0], false, false);
  auto r1 = __builtin_amdgcn_permlane16_swap(a[1], b[1], false, false);
  return (u32x4){r0[0], r1[0], r0[1], r1[1]};
}
__device__ __forceinline__ f32x4 mfma16(s16x8 a, s16x8 b, f32x4 c) {
  return __builtin_amdgcn_mfma_f32_16x16x32_bf16(__builtin_bit_cast(bf16x8, a), __builtin_bit_cast(bf16x8, b), c, 0, 0, 0);
}
#define TNT " nt"
__device__ __forceinline__ void mfma_acc(f32x4& c, s16x8 a, s16x8 b) { asm volatile("v_mfma_f32_16x16x32_bf16 %0, %1, %2, %0" : "+v"(c) : "v"(a), "v"(b)); }
#define MFMA_DONE() do { asm volatile("s_nop 7\n\ts_nop 7"); __builtin_amdgcn_sched_barrier(0); } while (0)
__device__ __forceinline__ s16x4 tr_read(LAS char* p) { return __builtin_amdgcn_ds_read_tr16_b64_v4i16((LAS s16x4*)p); }
__device__ __forceinline__ s16x8 cat8(s16x4 a, s16x4 b) { return __builtin_shufflevector(a, b, 0, 1, 2, 3, 4, 5, 6, 7); }
template <int OFF, int DOFF = 0> __device__ __forceinline__ void dma16(const void* base, unsigned voff, unsigned dst) {
  asm volatile("s_add_u32 m0, %0, %4\n\ts_nop 0\n\tglobal_load_lds_dwordx4 %1, %2 offset:%3" ::"s"(dst), "v"(voff), "s"(base), "i"(OFF), "i"(DOFF - OFF) : "scc");
}
template <int OFF, int DOFF> __device__ __forceinline__ void dma16_s(const void* base, unsigned voff, unsigned dst) {
  asm volatile("s_add_u32 m0, %0, %4\n\ts_nop 0\n\tglobal_load_lds_dwordx4 %1, %2 offset:%3" TNT ::"s"(dst), "v"(voff), "s"(base), "i"(OFF), "i"(DOFF - OFF) : "scc");
}
template <int DOFF, int NL> __device__ __forceinline__ void dma16_m(const void* base, unsigned voff, unsigned dst) {
  asm volatile("s_add_u32 m0, %0, %4\n\ts_bfm_b64 exec, %3, 0\n\tglobal_load_lds_dwordx4 %1, %2\n\ts_mov_b64 exec, -1"
               ::"s"(dst), "v"(voff), "s"(base), "i"(NL), "i"(DOFF) : "scc");
}
template <int NL> __device__ __forceinline__ void touch(const void* base, unsigned v, unsigned dst) {
  unsigned t;
  asm volatile("v_lshlrev_b32 %0, 3, %3\n\ts_mov_b32 m0, %2\n\ts_bfm_b64 exec, %4, 0\n\tglobal_load_lds_dword %0, %1\n\ts_mov_b64 exec, -1"
               : "=&v"(t) : "s"(base), "s"(dst), "v"(v), "i"(NL));
}
template <int L0> __device__ __forceinline__ void touch_hi(const void* base, unsigned v, unsigned dst) {
  asm volatile("s_mov_b32 m0, %1\n\ts_bfm_b64 exec, 4, %3\n\tglobal_load_lds_dword %2, %0\n\ts_mov_b64 exec, -1" :: "s"(base), "s"(dst), "v"(v), "i"(L0));
}
#define KNT " nt"
#define VNT " nt"
#define LD_HID(d, voff, base, OFF, MOD) asm volatile("global_load_dwordx4 %0, %1, %2 offset:%3" MOD " ; HID %0" : "=&v"(d) : "v"(voff), "s"(base), "i"(OFF))
#define LD_HIDB(d, voff, r, MOD) asm volatile("buffer_load_dwordx4 %0, %1, %2, 0 offen" MOD " ; HID %0" : "=&v"(d) : "v"(voff), "s"(r))
#define LANDED(x) asm volatile("; USE %0" : "+v"(x))
template <int H> __device__ __forceinline__ void colsum(float& cs, s16x8 w, int idx) {
  const u32x4 v = __builtin_bit_cast(u32x4, w);
  unsigned a;
  if constexpr (H == 0)
    asm volatile("s_set_gpr_idx_on %2, gpr_idx(SRC1)\n\tv_lshlrev_b32 %1, 16, %3 ; CSCHK %3 %4\n\ts_set_gpr_idx_off\n\tv_add_f32 %0, %0, %1"
                 : "+v"(cs), "=&v"(a) : "s"(idx), "v"(v[0]), "v"(v));
  else
    asm volatile("s_set_gpr_idx_on %2, gpr_idx(SRC1)\n\tv_and_b32 %1, 0xffff0000, %3 ; CSCHK %3 %4\n\ts_set_gpr_idx_off\n\tv_add_f32 %0, %0, %1"
                 : "+v"(cs), "=&v"(a) : "s"(idx), "v"(v[0]), "v"(v));
}
#define OPAQUE(x) asm volatile("" : "+v"(x))
#define WAIT_V(n) asm volatile("s_waitcnt vmcnt(%0)" ::"i"(n) : "memory")
#define WAIT_L0() __builtin_amdgcn_s_waitcnt(0xC07F)
#define WAIT_V0C() __builtin_amdgcn_s_waitcnt(0x0F70)
#define WAIT_VL(n) do { WAIT_L0(); WAIT_V(n); } while (0)
#define FENCE_C() asm volatile("" ::: "memory")
#define BAR() do { FENCE_C(); __builtin_amdgcn_s_barrier(); FENCE_C(); } while (0)
#define SCHED0() __builtin_amdgcn_sched_barrier(0)
#define RLX __ATOMIC_RELAXED, __HIP_MEMORY_SCOPE_AGENT
#define ST_SC1(v, off) __builtin_amdgcn_raw_buffer_store_b128(v, rs, off, 0, 16)
#define LD_SC1(off) __builtin_amdgcn_raw_buffer_load_b128(rs, off, 0, 16)

#define STAMP(i)
#define STAMP_OUT()
#define STEP_T(idx)
template <int V> struct IC { static constexpr int value = V; };
template <int N, int I = 0, class Fn> __device__ __forceinline__ void static_for(Fn&& f) {
  if constexpr (I < N) { f(IC<I>{}); static_for<N, I + 1>(f); }
}

__device__ __forceinline__ void wait_flags(GAS unsigned* p, unsigned v, unsigned tag) {
  if (__builtin_expect(!__all(v == tag), 0)) {
    for (unsigned spins = 0; spins < SPIN_CAP; ++spins) {
      __builtin_amdgcn_s_sleep(2);
      if (__all(__hip_atomic_load(p, RLX) == tag)) break;
    }
  }
  FENCE_C();
}

__device__ __forceinline__ unsigned lds_add(LAS char* p) { return __hip_atomic_fetch_add((LAS unsigned*)p, 1u, __ATOMIC_RELAXED, __HIP_MEMORY_SCOPE_WORKGROUP); }
__device__ __forceinline__ void lds_wait(LAS char* p, unsigned n) {
  FENCE_C();
  for (unsigned spins = 0; __hip_atomic_load((LAS unsigned*)p, __ATOMIC_RELAXED, __HIP_MEMORY_SCOPE_WORKGROUP) != n && spins < SPIN_CAP; ++spins) __builtin_amdgcn_s_sleep(1);
  FENCE_C();
}

constexpr int U_GS = 960;
constexpr int U_UP = 8 * U_GS + 16;
constexpr int U_WS = U_UP + 8 * U_GS;
constexpr int L_A = 61568;
constexpr int L_ST = L_A + 3 * 32768;
constexpr int L_JUNK = L_ST + 2048;
constexpr int L_SK = L_JUNK + 256;
constexpr int L_CNT = L_SK + 1024;
constexpr int L_END = L_CNT + 16;
constexpr int L_ALLOC = L_END;
constexpr int abuf(int u) { return L_A + (u % 3 == 0 ? 0 : u % 3 == 1 ? 65536 : 32768); }
#define CS0 0
#define XPOS 0
#define XN 2
#define DM0 7
constexpr int CODE_BYTES = 55 * 1024;
#define TW 8
#define W2T 11
#define SX0 1
#define SKA 4
#define SCL 24
#define UAUX 16
constexpr int S_X0 = SX0, S_X1 = S_X0 + 1, S_ST = S_X1 + 4, S_KA = SKA, S_KB = S_KA + 2, S_KV = S_KB + 4, S_V = S_KV, S_FL = S_V + 10, S_CL = SCL;
static_assert(S_ST + 4 <= S_KV || S_ST >= S_V + 7, "side jobs share registers");
constexpr bool w2t(int s) { return s >= W2T && !((s - W2T) & 1) && s < W2T + 14; }
constexpr bool wt(int s) { return s >= 0 && (s & ~1) + TW < 32; }
constexpr int extra(int s) {
  return wt(s) + w2t(s) + 3 * (s == 0) + (XPOS == 3 && s == -1) * XN + (s == S_X0 && !XPOS) + (s == S_X1 && (!XPOS || XN == 1)) + 3 * (s == S_KA) + 2 * (s == S_KB) + (s == S_ST + 3) +
         (s == S_V || s == S_V + 1) + (s == S_CL || s == S_CL + 1) + (s == S_V + 5 || s == S_V + 6) + (s == S_FL);
}
constexpr int L_CSP = 14 * 8192, L_CSF = L_CSP + 4096;
constexpr int C_TS = 32 + 4096, L_T = 143232;
constexpr int L_Z = L_T;
constexpr int C_US = 256 + 4096, L_U = L_T - 4 * C_US;
#define CSP 20
constexpr int CSPLIT = CSP;
static_assert(L_CSP >= abuf(14) && L_T >= abuf(13) && L_T + 4 * C_TS <= L_ST && L_U >= 15 * 8192 && L_T >= 16 * 8192, "lds map");
constexpr int D_SLOT = 8192, N1 = 48, N2 = 16, NT = N1 + N2;
#define PRE 1
#define STR 2
#define LND 6
constexpr int staged(int c) { return 2 * PRE + STR * c < 15 ? 2 * PRE + STR * c : 15; }
static_assert(staged(CSPLIT) == 15 && staged(LND) >= 8, "");

template <int LO, int HI, class Hook>
__device__ __forceinline__ void conv_part(LAS char* tp, LAS char* gp, LAS char* zp, bool last, Hook&& hook) {
  constexpr int NH = HI / 2 + 1;
  s16x8 Fh[NH];
#pragma unroll
  for (int n = 0; n < NH; ++n) Fh[n] = cat8(tr_read(tp + n * 256), tr_read(tp + n * 256 - 32));
  WAIT_VL(0); BAR();
  unsigned ga = (unsigned)(size_t)gp, za = (unsigned)(size_t)zp;
  OPAQUE(ga); OPAQUE(za);
  s16x8 G[HI + 1];
#define G_RD(p) G[p] = cat8(tr_read((LAS char*)(size_t)(ga + (p) * 128)), tr_read((LAS char*)(size_t)(ga + (p) * 128 + 32)))
  const f32x4 zero = {0.f, 0.f, 0.f, 0.f};
  f32x4 z[2];
  float ta[4], tb[4], pr[4] = {0.f, 0.f, 0.f, 0.f}, u0, u1;
  auto keep = [&]() {
#pragma unroll
    for (int i = 0; i < 4; ++i) { ta[i] = z[0][i]; tb[i] = z[1][i]; }
  };
  if constexpr (LO == 0) { G_RD(0); G_RD(1); }
  else {
#pragma unroll
    for (int p = 1; p < LO; p += 2) G_RD(p);
    SCHED0();
    static_for<LO / 2>([&](auto J_) { constexpr int j = J_.value;
      z[j & 1] = mfma16(G[2 * j + 1], Fh[LO / 2 - 1 - j], j < 2 ? zero : z[j & 1]);
      G_RD(2 * j);
      SCHED0(); });
    G_RD(LO); G_RD(LO + 1);
    keep();
  }
  auto fin = [&](auto C_, auto K_) { constexpr int c = C_.value, k = K_.value, q = 3 - (k + 1) / 2;
    if constexpr (c < LO) {
      if constexpr (k == 0) { pr[3] = ta[3] + tb[3]; pr[2] = ta[2] + tb[2]; OPAQUE(pr[3]); OPAQUE(pr[2]); }
      if constexpr (k == 2) { pr[2] += dpp<0x13C>(pr[3]); pr[1] = ta[1] + tb[1]; OPAQUE(pr[2]); OPAQUE(pr[1]); }
      if constexpr (k == 4) { pr[1] += dpp<0x13C>(pr[2]); OPAQUE(pr[1]); }
    } else if constexpr (k == 6) {
      const unsigned o = pk_bf16(ta[0], tb[0]);
      *(LAS u16*)(size_t)(za + c * 512) = (u16)o;
      *(LAS u16*)(size_t)(za + c * 512 + 512) = (u16)(o >> 16);
    } else if constexpr (k & 1) {
      ta[q] += dpp<0x13C>(u0); tb[q] += dpp<0x13C>(u1); OPAQUE(ta[q]); OPAQUE(tb[q]);
    } else {
      u0 = last ? pr[q] : ta[q]; u1 = last ? ta[q] : tb[q]; pr[q] = tb[q]; OPAQUE(u0); OPAQUE(u1);
    }
  };
  SCHED0();
  static_for<(HI - LO + 1) / 2>([&](auto C_) {
    constexpr int c = LO + 2 * C_.value, nm = c + 2, g0 = nm > 10 ? 4 : nm > 7 ? nm - 7 : 0;
    hook(IC<c>{});
    static_for<nm>([&](auto J_) { constexpr int j = J_.value, n = c / 2 - j / 2;
      z[j & 1] = mfma16(G[c + (j & 1) - 2 * n], Fh[n], j < 2 ? zero : z[j & 1]);
      if constexpr (j < 2 && c + 2 <= HI) G_RD(c + 2 + j);
      if constexpr (c > 0) static_for<7>([&](auto K_) { if constexpr (j == (g0 + K_.value < nm ? g0 + K_.value : nm - 1)) fin(IC<c - 2>{}, K_); });
      if constexpr (j == nm / 2) hook(IC<c + 1>{});
      SCHED0(); });
    keep();
  });
  static_for<7>([&](auto K_) { fin(IC<HI - 1>{}, K_); });
}

__global__ __launch_bounds__(512, 2) void k_all(const u16* __restrict__ x, const u16* __restrict__ kk, const u16* __restrict__ vv,
                                                const u16* __restrict__ shk, const u16* __restrict__ ssmk, const u16* __restrict__ ssmd,
                                                const u16* __restrict__ w1, const u16* __restrict__ w3, const u16* __restrict__ w2,
                                                u16* __restrict__ out, char* __restrict__ ws) {
  extern __shared__ __attribute__((aligned(16))) char smem[];
  LAS char* sm = (LAS char*)smem;
  int tid = threadIdx.x, lane = tid & 63;
  const int wid = __builtin_amdgcn_readfirstlane(tid >> 6);
  const int bid = blockIdx.x, xcd = bid & 7, rr = bid >> 3;
  const int mb = (xcd & 1) * 4 + (rr & 3), nb = (xcd >> 1) * 8 + (rr >> 2);
  const int wm = wid & 3, wn = wid >> 2;
  int g = lane >> 4, fr = lane & 15, qq = fr >> 2, pp = lane & 3;
  const __amdgpu_buffer_rsrc_t rs = __builtin_amdgcn_make_buffer_rsrc(ws, 0, WS_END, 0x00020000);
  const unsigned lds0 = (unsigned)(size_t)sm;
  STAMP(0);

  const char* gA[4];
#pragma unroll
  for (int i = 0; i < 4; ++i) gA[i] = (const char*)x + (size_t)(mb * 256 + i * 64 + wid * 8) * (D_ * 2);
  const char* gG = (const char*)w1 + ((size_t)(wid * 4) * F_ + nb * 112) * 2;
  const char* gU = (const char*)w3 + ((size_t)(wid * 4) * F_ + nb * 112) * 2;
  const unsigned vA = (lane >> 3) * (D_ * 2) + (((lane & 7) ^ ((wid & 1) * 4 + g)) * 16);
  const int tq = min(wid * 4 + (lane & 3), 27);
  const unsigned vB = lane < 56 ? (lane / 14) * (F_ * 2) + (lane % 14) * 16 : lane < 60 ? (tq / 14) * (F_ * 2) + (tq % 14) * 128 : (wid * 4 + (lane & 3)) * 2048;
  const unsigned wto = (TW * 32 + rr * 2) * (F_ * 2) + (xcd >> 1) * 1792;
  const unsigned lA = lds0 + wid * 1024, lB = lds0 + wid * U_GS;
#define A_DMA(q, i) dma16<((q) >> 1) * 128, abuf((q) >> 1) + (((q) & 1) * 2 + (i)) * 8192>(gA[((q) & 1) * 2 + (i)], vA, lA)
#define W_DMA(slot, i, NL) do { \
    if (i == 0) { dma16_m<(slot) * U_WS, NL>(gG, vB, lB); gG += 32 * F_ * 2; } \
    else { dma16_m<(slot) * U_WS + U_UP, NL>(gU, vB, lB); gU += 32 * F_ * 2; } } while (0)
#define G1_DMA() do { A_DMA(0, 0); A_DMA(0, 1); A_DMA(1, 0); A_DMA(1, 1); W_DMA(0, 0, 56); W_DMA(0, 1, 56); W_DMA(1, 0, 56); W_DMA(1, 1, 56); } while (0)
#define G2_DMA() do { A_DMA(2, 0); A_DMA(2, 1); A_DMA(3, 0); A_DMA(3, 1); W_DMA(2, 0, 56); W_DMA(2, 1, 56); } while (0)
  const int tb = bid >> 4, cb = bid & 15;
  f32x4 acc[4][7];
  u32x4 d[2], kr[5], vr[2];
  unsigned uo[2][4];
  const char* xrow = (const char*)x + (bid * 8 + wid) * (D_ * 2);
#define X_LD() do { LD_HID(d[0], lane * 16, xrow, 0, ""); if constexpr (XN == 2) LD_HID(d[1], lane * 16, xrow, 1024, ""); } while (0)
  if constexpr (XPOS == 1) X_LD();
  G1_DMA();
  if constexpr (XPOS == 2) X_LD();
#pragma unroll
  for (int i = 0; i < 28; ++i) { acc[i & 3][i >> 2] = (f32x4){0.f, 0.f, 0.f, 0.f}; OPAQUE(acc[i & 3][i >> 2]); }
  if (tid == 0) *(LAS u32x4*)(sm + L_CNT) = (u32x4){0u, 0u, 0u, 0u};
  unsigned ra[2][2], rw[2];
  ra[0][0] = lds0 + L_A + (wm * 64 + fr) * 128 + ((g ^ (fr >> 1)) * 16);
  ra[0][1] = ra[0][0] ^ 64;
  ra[1][0] = ra[0][0] + 65536; ra[1][1] = ra[0][1] + 65536;
  rw[0] = lds0 + 2 * g * U_GS + qq * 224 + wn * 112 + pp * 8;
  rw[1] = lds0 + 2 * g * U_GS + qq * 224 + wn * 112 + 96 + (pp & 1) * 8 + (pp >> 1) * U_UP;
  OPAQUE(ra[0][0]); OPAQUE(ra[0][1]); OPAQUE(ra[1][0]); OPAQUE(ra[1][1]); OPAQUE(rw[0]); OPAQUE(rw[1]);
  SCHED0();
  G2_DMA();
  if constexpr (XPOS == 3) X_LD();
#define U_RD(i, X, Wl, Wh, t) do { \
    if constexpr (i >= 2 && i < 6) X[i - 2] = *(LAS s16x8*)(size_t)(ra[((t) >> 1) % 3 == 1][(t) & 1] + (((t) >> 1) % 3 == 2 ? 32768 : 0) + (i - 2) * 2048); \
    else { constexpr int j_ = i < 2 ? i : i - 4, c_ = j_ >> 1; \
      LAS char* p_ = (LAS char*)(size_t)(rw[c_ == 6] + ((t) & 3) * U_WS + (c_ == 6 ? 0 : (c_ >> 1) * 32 + (c_ & 1) * U_UP) + (j_ & 1) * U_GS); \
      if constexpr (j_ & 1) Wh[c_] = tr_read(p_); else Wl[c_] = tr_read(p_); } } while (0)
  float cs[7] = {0.f, 0.f, 0.f, 0.f, 0.f, 0.f, 0.f};
  s16x8 X[2][4];
  s16x4 Wl[2][7], Wh[2][7];
  WAIT_V(8 + (XPOS >= 2) * XN); BAR();
  STAMP(1);
  static_for<18>([&](auto I_) { constexpr int i = I_.value; U_RD(i, X[0], Wl[0], Wh[0], 0); });
  SCHED0(); WAIT_VL(6 + (XPOS >= 2) * XN); BAR(); SCHED0();

  const int cch = wid & 3;
  unsigned tag = 0, fu = 0, dsk = 0, ep;
  u32x2 skr;
  u32x4 sg;
#define U_DMA(s, i) do { \
    if constexpr ((s) < 28 && (i) < 2) W_DMA((s) & 3, (i), 56); \
    if constexpr ((s) < 27 && (i) >= 2) A_DMA((s) + 5, (i) - 2); \
    if constexpr ((s) == 27 && (i) >= 2) { OPAQUE(tid); const unsigned l_ = tid & 63; \
      dma16_s<((i) - 2) * 1024, ((i) - 2) * 1024 + L_T + 32>((const char*)ssmk + (size_t)(bid * 4 + cch) * 4096 + wn * 2048, l_ * 16, lds0 + cch * C_TS + wn * 2048); } } while (0)
  float ax, ay, bx, by, px, py, qx, qy, mean;
  f32x4 tx, ty;
#define PIN(a, b) do { OPAQUE(a); OPAQUE(b); } while (0)
#define UP2(a, b, v) do { a = bflo(v); b = bfhi(v); PIN(a, b); } while (0)
  auto side = [&](auto S_c, auto I_) { constexpr int s = S_c.value, i = I_.value;
    constexpr bool slot = (i & 3) == 0 || (i & 3) == 3;
    if constexpr (s == 4) {
      if constexpr (i == 0) { LANDED(ep); LANDED(skr); tag = __builtin_amdgcn_readfirstlane(ep) + 0x9E3779B9u; }
      if constexpr (i == 3) UP2(tx[0], tx[1], skr[0]);
      if constexpr (i == 4) UP2(tx[2], tx[3], skr[1]);
      if constexpr (i == 8) { OPAQUE(tid); *(LAS f32x4*)(sm + L_SK + (tid & 63) * 16) = tx; }
    }
    if constexpr (slot && s >= S_ST && s < S_ST + 4) {
      constexpr int n = (s - S_ST) * 14 + (i >> 1), r = n < 22 ? n - 16 : n - 46;
      if constexpr (n == 0) { LANDED(d[0]); LANDED(d[1]); }
      if constexpr (n == 22) PIN(d[0], d[1]);
      if constexpr (n < 16) {
        if constexpr (!(n & 1)) UP2(ax, ay, d[n >> 3][(n >> 1) & 3]);
        else { if constexpr (n == 1) { px = ax; py = ay; } else { px += ax; py += ay; } PIN(px, py); }
      }
      if constexpr (n >= 22 && n < 46) {
        constexpr int j = (n - 22) / 3, k = (n - 22) % 3;
        if constexpr (k == 0) UP2(ax, ay, d[j >> 2][j & 3]);
        if constexpr (k == 1) { ax -= mean; ay -= mean; PIN(ax, ay); }
        if constexpr (k == 2) { px = j ? fmaf(ax, ax, px) : ax * ax; py = j ? fmaf(ay, ay, py) : ay * ay; PIN(px, py); }
      }
      if constexpr (r == 0) { px += py; px += dpp<0xB1>(px); OPAQUE(px); }
      if constexpr (r == 1) { px += dpp<0x4E>(px); OPAQUE(px); }
      if constexpr (r == 2) { px += dpp<0x141>(px); OPAQUE(px); }
      if constexpr (r == 3) { px += dpp<0x140>(px); OPAQUE(px); }
      if constexpr (r == 4) { px = add16(px, px); OPAQUE(px); }
      if constexpr (r == 5) { px = add32(px, px) * (1.0f / D_); OPAQUE(px); if constexpr (n < 22) mean = px; }
      if constexpr (r == 6) { px = rbf(px) + 1e-5f; OPAQUE(px); }
      if constexpr (r == 7) { px = __builtin_amdgcn_sqrtf(rbf(px)); OPAQUE(px); }
      if constexpr (r == 8) { px = rbf(px); mean = rbf(mean); PIN(px, mean); }
      if constexpr (r == 9) {
        OPAQUE(tid);
        ST_SC1(((u32x4){__float_as_uint(mean), tag, __float_as_uint(px), tag}), WS_ST + (bid * 8 + wid) * 16 + (tid & 63) * 0x2000000);
      }
    }
    if constexpr (i == 27 && s >= S_KV - 1 && s < S_KV + 3) {
      OPAQUE(tid);
      tx = *(LAS f32x4*)(sm + L_SK + (tid & 7) * 128 + (s + 1 - S_KV) * 32);
      ty = *(LAS f32x4*)(sm + L_SK + (tid & 7) * 128 + (s + 1 - S_KV) * 32 + 16);
    }
    if constexpr (slot && s >= S_KV && s < S_KV + 4) {
      constexpr int e = s - S_KV, k = i >> 1;
      if constexpr (e == 0 && k == 0) { LANDED(kr[0]); LANDED(kr[1]); LANDED(kr[2]); LANDED(kr[3]); LANDED(kr[4]); }
      if constexpr (k == 0) UP2(ax, ay, kr[4][e]);
      if constexpr (k == 1) UP2(bx, by, kr[3][e]);
      if constexpr (k == 2) { qx = ax * tx[0]; qy = ay * ty[0]; PIN(qx, qy); }
      if constexpr (k == 3) { px = bx * tx[0]; py = by * ty[0]; PIN(px, py); }
      if constexpr (k == 4) UP2(ax, ay, kr[2][e]);
      if constexpr (k == 5) { qx = fmaf(bx, tx[1], qx); qy = fmaf(by, ty[1], qy); PIN(qx, qy); }
      if constexpr (k == 6) { px = fmaf(ax, tx[1], px); py = fmaf(ay, ty[1], py); PIN(px, py); }
      if constexpr (k == 7) UP2(bx, by, kr[1][e]);
      if constexpr (k == 8) { qx = fmaf(ax, tx[2], qx); qy = fmaf(ay, ty[2], qy); PIN(qx, qy); }
      if constexpr (k == 9) { px = fmaf(bx, tx[2], px); py = fmaf(by, ty[2], py); PIN(px, py); }
      if constexpr (k == 10) UP2(ax, ay, kr[0][e]);
      if constexpr (k == 11) { qx = fmaf(bx, tx[3], qx); qy = fmaf(by, ty[3], qy); PIN(qx, qy); }
      if constexpr (k == 12) { px = fmaf(ax, tx[3], px); py = fmaf(ay, ty[3], py); PIN(px, py); }
      if constexpr (k == 13) { uo[0][e] = pk_bf16(px, py); uo[1][e] = pk_bf16(qx, qy); PIN(uo[0][e], uo[1][e]); }
    }
    if constexpr (slot && s >= S_V + 5 && s < S_V + 7) {
      constexpr int e = (s - S_V - 5) * 2 + (i >> 1) / 7, k = (i >> 1) % 7;
      if constexpr (e == 0 && k == 0) { LANDED(vr[0]); LANDED(vr[1]); }
      if constexpr (k == 0) UP2(ax, ay, uo[0][e]);
      if constexpr (k == 1) UP2(px, py, vr[0][e]);
      if constexpr (k == 2) UP2(bx, by, uo[1][e]);
      if constexpr (k == 3) UP2(qx, qy, vr[1][e]);
      if constexpr (k == 4) { px *= ax; py *= ay; PIN(px, py); }
      if constexpr (k == 5) { qx *= bx; qy *= by; PIN(qx, qy); }
      if constexpr (k == 6) { uo[0][e] = pk_bf16(px, py); uo[1][e] = pk_bf16(qx, qy); PIN(uo[0][e], uo[1][e]); }
      if constexpr (i == 27) {
        OPAQUE(tid);
        __builtin_amdgcn_raw_buffer_store_b128((u32x4){uo[0][e - 1], uo[0][e], uo[1][e - 1], uo[1][e]}, rs, (tid & 7) * (2 * S_ * 8) + (tid >> 3) * 16,
                                               WS_U + ((cb * 16 + (e >> 1)) * S_ + tb * 128) * 8, UAUX);
      }
    }
    if constexpr (i == 20) {
      if constexpr (wt(s)) touch_hi<56>((const char*)(s & 1 ? w3 : w1) + wto + (s & ~1) * 32 * (F_ * 2), vB, lds0 + L_JUNK);
      if constexpr (s == S_CL || s == S_CL + 1) { OPAQUE(tid); __builtin_amdgcn_raw_buffer_store_b128((u32x4){~0u, ~0u, ~0u, ~0u}, rs, tid * 16, WS_EX + bid * 16384 + (s - S_CL) * 8192, 16); }
      if constexpr (s == 0) {
        const char* pc;
        asm volatile("s_getpc_b64 %0" : "=s"(pc));
        touch<2>(pc + min(rr * 8 + wid, CODE_BYTES / 256 - 1) * 256, vB, lds0 + L_JUNK);
        OPAQUE(tid);
        asm volatile("global_load_dwordx2 %0, %1, %2 ; HID %0" : "=&v"(skr) : "v"((tid & 63) * 8), "s"((const char*)shk + cb * 512));
        asm volatile("v_mov_b32 %0, 0\n\tglobal_load_dword %0, %0, %1 sc1 ; HID %0" : "=&v"(ep) : "s"(ws + WS_EP));
      }
      if constexpr ((s == S_X0 && !XPOS) || (s == S_X1 && (!XPOS || XN == 1))) { OPAQUE(tid); LD_HID(d[s - S_X0], (tid & 63) * 16, xrow, (s - S_X0) * 1024, ""); }
      if constexpr (s == S_KA || s == S_KB) {
        OPAQUE(tid);
        const __amdgpu_buffer_rsrc_t rk = __builtin_amdgcn_make_buffer_rsrc((void*)kk, 0, S_ * D_ * 2, 0x00020000);
        const unsigned o = (tid >> 3) * (D_ * 4) + (tid & 7) * 16 + (tb * 128 - 3) * (D_ * 2) + cb * 128;
        if constexpr (s == S_KA) { LD_HIDB(kr[4], o + 4 * D_ * 2, rk, KNT); LD_HIDB(kr[2], o + 2 * D_ * 2, rk, KNT); LD_HIDB(kr[0], o, rk, KNT); }
        else { LD_HIDB(kr[3], o + 3 * D_ * 2, rk, KNT); LD_HIDB(kr[1], o + D_ * 2, rk, KNT); }
      }
      if constexpr (s == S_V || s == S_V + 1) {
        OPAQUE(tid);
        LD_HID(vr[s - S_V], (tid >> 3) * (D_ * 4) + (tid & 7) * 16, (const char*)vv + tb * 128 * (D_ * 2) + cb * 128, (s - S_V) * (D_ * 2), VNT);
      }
      if constexpr (s == S_FL) { OPAQUE(tid); __builtin_amdgcn_raw_buffer_store_b32(tag, rs, WS_FU + (cb * 16 + tb) * 128 + min(tid, 1) * 0x2000000, 0, 16); }
    }
    if constexpr (i == 24) {
      if constexpr (w2t(s)) touch_hi<60>((const char*)w2 + ((s - W2T) / 2 * 16 + (bid >> 4)) * (32 * D_ * 2) + (bid & 15) * 128, vB, lds0 + L_JUNK);
    }
  };
  static_for<31>([&](auto S_c) {
    constexpr int s = S_c.value, b = s & 1;
    STEP_T(s);
    if constexpr (s == 28) {
      OPAQUE(tid);
      sg = LD_SC1(WS_ST + (mb * 256 + (tid & 255)) * 16);
      fu = __hip_atomic_load((GAS unsigned*)(ws + WS_FU + ((bid >> 4) * 16 + (tid & 15)) * 128), RLX);
      dsk = __hip_atomic_load((GAS u16*)(ssmd + bid * 4 + (tid & 3)), RLX);
    }
    SCHED0();
    static_for<28>([&](auto I_) { constexpr int i = I_.value, c = i >> 2, m = i & 3;
      mfma_acc(acc[m][c], cat8(Wl[b][c], Wh[b][c]), X[b][m]);
      if constexpr (i < 18) U_RD(i, X[b ^ 1], Wl[b ^ 1], Wh[b ^ 1], s + 1);
      if constexpr ((i & 3) == 3 && i >= DM0 && i < DM0 + 16) U_DMA(s, (i - DM0) >> 2);
      if constexpr (s == 0 && i < 6 && i % 3 != 2) { if constexpr (i < 2) W_DMA(3, i, 56); else A_DMA(4, i - 3); }
      side(S_c, I_);
      if constexpr ((m == 1 || m == 2) && s >= CS0) colsum<m - 1>(cs[c], cat8(Wl[b][c], Wh[b][c]), wm);
      if constexpr (s == 30 && i >= 14) colsum<i & 1>(cs[(i - 14) >> 1], cat8(Wl[1][(i - 14) >> 1], Wh[1][(i - 14) >> 1]), wm);
      SCHED0(); });
    if constexpr (s < 30) {
      WAIT_VL((s == 1 ? 10 - extra(-1) : s == 28 ? 7 : s == 29 ? 5 : 8) + extra(s - 2) + extra(s - 1) + extra(s));
      BAR(); SCHED0();
    }
  });
  STEP_T(31);
  OPAQUE(tid); lane = tid & 63; g = lane >> 4; fr = lane & 15; qq = fr >> 2; pp = lane & 3;
  const int mb2 = mb * 2 + ((rr >> 2) & 1);
  const int kh = xcd >> 2, nb2 = ((xcd >> 1) & 1) * 4 + (rr >> 3), pbid = bid ^ 4;

  const char* wB = (const char*)w2 + ((size_t)(cch * 8) * D_ + nb2 * 128) * 2;
  const char* gB = wB + (size_t)(kh * NT + wn * PRE) * (32 * D_ * 2);
  const unsigned v2 = g * (D_ * 2) + ((fr ^ ((g | ((wid & 1) << 2)) << 1)) * 16);
  const unsigned dB = lds0 + cch * 2048;
#define D_STAGE(d, o) do { dma16<0, (o)>(gB, v2, d); dma16<0, (o) + 1024>(gB + 4 * D_ * 2, v2, d); gB += 32 * D_ * 2; } while (0)
  u32x4 ur[2];
  f32x4 cv[7];
  f32x2 st[4];
  {
    float r[8];
    SCHED0();
    static_for<28>([&](auto I_) { constexpr int i = I_.value, c = i >> 2, m = i & 3;
      mfma_acc(acc[m][c], cat8(Wl[1][c], Wh[1][c]), X[1][m]);
      if constexpr (i == 1) {
        *(LAS float*)(sm + L_CSP + wid * 512 + lane * 4) = add16(add32(cs[0], cs[1]), add32(cs[2], cs[3]));
        *(LAS float*)(sm + L_CSP + wid * 512 + 256 + lane * 4) = add16(add32(cs[4], cs[5]), add32(cs[6], 0.f));
      }
      if constexpr (i == 4) {
        for (unsigned spins = 0; !__all(sg[1] == tag && sg[3] == tag) && spins < SPIN_CAP; ++spins) {
          __builtin_amdgcn_s_sleep(2); FENCE_C();
          sg = LD_SC1(WS_ST + (mb * 256 + (tid & 255)) * 16);
        }
        *(LAS u32x2*)(sm + L_ST + (tid & 255) * 8) = (u32x2){sg[0], sg[2]};
        wait_flags((GAS unsigned*)(ws + WS_FU + ((bid >> 4) * 16 + fr) * 128), fu, tag);
#pragma unroll
        for (int k = 0; k < 2; ++k) ur[k] = __builtin_amdgcn_raw_buffer_load_b128(rs, WS_U + bid * 16384 + tid * 32 + k * 16, 0, UAUX);
      }
      if constexpr (i == 6) {
        if (wid < 4 && lane == wid) {
          LAS char* p = sm + L_T + wid * C_TS;
          *(LAS u32x4*)p = (u32x4){0u, 0u, 0u, 0u}; *(LAS u32x4*)(p + 16) = (u32x4){0u, 0u, 0u, 0u};
          *(LAS u16*)(p + 32) = (u16)pk_bf16(bflo(*(LAS u16*)(p + 32)) + bflo(dsk), 0.f);
        }
      }
      if constexpr (i == 10) {
        WAIT_L0(); BAR();
#pragma unroll
        for (int k = 0; k < 8; ++k) r[k] = *(LAS float*)(sm + L_CSP + wn * 2048 + k * 256 + lane * 4);
#pragma unroll
        for (int k = 0; k < 4; ++k) st[k] = *(LAS f32x2*)(sm + L_ST + (wm * 64 + k * 16 + fr) * 8);
      }
      if constexpr (i == 16) {
        LAS char* q = sm + L_CSF + wid * 512;
        *(LAS float*)(q + (((g & 1) << 1) | (g >> 1)) * 64 + fr * 4) = (r[0] + r[2]) + (r[4] + r[6]);
        *(LAS float*)(q + (((g & 1) << 1) | (g >> 1)) * 64 + 256 + fr * 4) = (r[1] + r[3]) + (r[5] + r[7]);
#pragma unroll
        for (int c2 = 0; c2 < 7; ++c2) cv[c2] = *(LAS f32x4*)(q + c2 * 64 + g * 16);
      }
      if constexpr (i == 23 && PRE > 0) D_STAGE(dB + wn * (PRE * D_SLOT), 0);
      if constexpr (i == 20) {
        if (tid < 64) *(LAS u32x4*)(sm + L_U + g * C_US + fr * 16) = (u32x4){0u, 0u, 0u, 0u};
      }
      SCHED0(); });
  }
  MFMA_DONE();
  STEP_T(32);
  STAMP(2);

  {
    float ea[4], s2[4];
#pragma unroll
    for (int m = 0; m < 4; ++m) { ea[m] = -1.44269504089f * __builtin_amdgcn_rcpf(st[m][1]); s2[m] = st[m][1] * st[m][1]; }
    auto sw = [](f32x2 a, f32x2 b, float e, float q) {
      const f32x2 t = a * e;
      const f32x2 d = (f32x2){__builtin_amdgcn_exp2f(t[0]), __builtin_amdgcn_exp2f(t[1])} * q + q;
      const f32x2 h = a * b * (f32x2){__builtin_amdgcn_rcpf(d[0]), __builtin_amdgcn_rcpf(d[1])};
      return pk_bf16(h[0], h[1]);
    };
    const unsigned hb = WS_H + ((nb * 14 + wn * 7) * S_ + mb * 256 + wm * 64) * 16;
    const unsigned ho = ((g >> 1) * S_ + (g & 1) * 16 + fr) * 16;
#pragma unroll
    for (int P = 0; P < 3; ++P)
#pragma unroll
      for (int mp = 0; mp < 2; ++mp) {
        u32x2 h[2];
#pragma unroll
        for (int i = 0; i < 2; ++i) {
          const int m = 2 * mp + i;
          const f32x4 ga = acc[m][2 * P] - st[m][0] * cv[2 * P], up = acc[m][2 * P + 1] - st[m][0] * cv[2 * P + 1];
          h[i] = (u32x2){sw(ga.xy, up.xy, ea[m], s2[m]), sw(ga.zw, up.zw, ea[m], s2[m])};
        }
        ST_SC1(swap16(h[0], h[1]), hb + ho + (2 * P * S_ + mp * 32) * 16);
        if (P * 2 + mp == 0 && PRE > 1) D_STAGE(dB + wn * (PRE * D_SLOT), D_SLOT);
        if (P * 2 + mp == 1 && PRE > 2) D_STAGE(dB + wn * (PRE * D_SLOT), 2 * D_SLOT);
        if (P * 2 + mp == 2 && PRE > 3) D_STAGE(dB + wn * (PRE * D_SLOT), 3 * D_SLOT);
        SCHED0();
      }
    u32x2 t6[2];
#pragma unroll
    for (int mp = 0; mp < 2; ++mp) {
      const float e = lane < 32 ? ea[2 * mp] : ea[2 * mp + 1], q = lane < 32 ? s2[2 * mp] : s2[2 * mp + 1];
      f32x4 ga = acc[2 * mp][6] - st[2 * mp][0] * cv[6], up = acc[2 * mp + 1][6] - st[2 * mp + 1][0] * cv[6];
#pragma unroll
      for (int j = 0; j < 4; ++j) {
        auto r = __builtin_amdgcn_permlane32_swap(__float_as_uint(ga[j]), __float_as_uint(up[j]), false, false);
        ga[j] = __uint_as_float(r[0]); up[j] = __uint_as_float(r[1]);
      }
      t6[mp] = (u32x2){sw(ga.xy, up.xy, e, q), sw(ga.zw, up.zw, e, q)};
    }
    ST_SC1(swap16(t6[0], t6[1]), hb + (6 * S_ + ((g & 1) * 2 + (g >> 1)) * 16 + fr) * 16);
  }
#pragma unroll
  for (int c = 0; c < 4; ++c) {
    const unsigned sel = (c & 1) ? 0x07060302u : 0x05040100u;
    *(LAS u32x2*)(sm + L_U + c * C_US + 256 + tid * 8) =
        (u32x2){__builtin_amdgcn_perm(ur[0][2 + (c >> 1)], ur[0][c >> 1], sel), __builtin_amdgcn_perm(ur[1][2 + (c >> 1)], ur[1][c >> 1], sel)};
  }

  LAS char* tp = sm + L_T + cch * C_TS + 32 + (-12 + 4 * pp + 4 * (31 - 8 * g - qq)) * 2;
  LAS char* gp = sm + L_U + cch * C_US + 256 + (16 * pp - 112 + 4 * (8 * g + qq)) * 2;
  LAS char* zp = sm + L_Z + lane * 8 + cch * 2;
  if (wid < 4) {
    gB = wB + (size_t)(kh * NT + 2 * PRE) * (32 * D_ * 2);
#define D_GROUP(j) do { const unsigned d_ = dB + ((j) & 3) * (4 * D_SLOT); \
      D_STAGE(d_, 0); D_STAGE(d_, D_SLOT); D_STAGE(d_, 2 * D_SLOT); D_STAGE(d_, 3 * D_SLOT); } while (0)
    conv_part<0, CSPLIT - 1>(tp, gp, zp, lane == 63, [&](auto P_) { constexpr int p = P_.value;
      if constexpr (p == 0) STAMP(3);
      if constexpr (p == 0) __builtin_amdgcn_raw_buffer_store_b32(tag, rs, WS_FH + (mb * 32 + nb) * 128 + min(tid, 1) * 0x2000000, 0, 16);
      if constexpr (p == LND) { STAMP(8); WAIT_V(2 * (staged(p) - 8)); if (lane == 0) lds_add(sm + L_CNT); STAMP(9); }
      static_for<staged(p + 1) - staged(p)>([&](auto I_) { D_STAGE(dB, (staged(p) + I_.value) * D_SLOT); }); });
    STAMP(4);
    FENCE_C();
    if (lane == 0) lds_add(sm + L_CNT + 8);
    lds_wait(sm + L_CNT + 8, 8);
    STAMP(5);
    D_STAGE(dB, 15 * D_SLOT);
    {
      u32x4 o[4];
#pragma unroll
      for (int i = 0; i < 4; ++i) o[i] = *(LAS u32x4*)(sm + L_Z + i * 4096 + tid * 16);
#pragma unroll
      for (int i = 0; i < 4; ++i) ST_SC1(o[i], WS_Z + bid * 16384 + i * 4096 + tid * 16);
    }
    STAMP(6);
    WAIT_V(6); BAR();
    STAMP(7);
    D_GROUP(4); D_GROUP(5);
    WAIT_V(20); BAR();
    D_GROUP(6);
    for (int j = 4; j < NT / 4 - 3; ++j) {
      WAIT_V(16); BAR();
      if (j == 4 && tid == 0) __hip_atomic_store((GAS unsigned*)(ws + WS_FZ + bid * 128), tag, RLX);
      if (j + 3 == N1 / 4) gB = wB + (size_t)N1 * (32 * D_ * 2);
      D_GROUP(j + 3);
    }
    WAIT_V(16); BAR();
    WAIT_V(8); BAR();
    WAIT_V(0); BAR();
    if (bid == 0 && wid == 3) {
#pragma unroll
      for (int i = 0; i < 4; ++i) { GAS unsigned* fp = (GAS unsigned*)(ws + WS_FU + (i * 64 + lane) * 128); wait_flags(fp, __hip_atomic_load(fp, RLX), tag); }
      if (lane == 0) __hip_atomic_store((GAS unsigned*)(ws + WS_EP), tag, RLX);
    }
    STAMP_OUT();
    return;
  }

  __builtin_amdgcn_s_setprio(1);
  conv_part<CSPLIT, 31>(tp, gp, zp, lane == 63, [&](auto P_) { if constexpr (P_.value == CSPLIT) STAMP(3); });
  FENCE_C();
  if (lane == 0) lds_add(sm + L_CNT + 8);
  STAMP(4);
  const int row0 = mb2 * 128 + kh * 64 + cch * 16 + fr;
  u32x4 xv[4];
  {
    GAS unsigned* fp = (GAS unsigned*)(ws + (lane < 19 ? WS_FH + (mb * 32 + kh * 13 + lane) * 128 : WS_FH + (mb * 32 + (nb ^ 16)) * 128));
    wait_flags(fp, __hip_atomic_load(fp, RLX), tag);
  }
  STAMP(5);
  const unsigned vK = WS_H + (g * S_ + row0) * 16, vS = vK ^ 1024;
  unsigned ko = kh * NT * (4 * S_ * 16);
  u32x4 Hf[8][2];
#define H_LD(t, m) Hf[(t) & 7][m] = __builtin_amdgcn_raw_buffer_load_b128(rs, (m) ? vS : vK, ko, 16)
  static_for<8>([&](auto I_) { H_LD(I_.value, 0); H_LD(I_.value, 1); ko += 4 * S_ * 16; });
  unsigned rb[2][8];
#pragma unroll
  for (int c = 0; c < 8; ++c) {
    rb[0][c] = lds0 + (8 * g + qq) * 256 + ((c ^ (qq | ((g & 1) << 2))) * 32) + pp * 8;
    rb[1][c] = rb[0][c] + 8 * D_SLOT; OPAQUE(rb[0][c]); OPAQUE(rb[1][c]);
  }
  s16x4 Vl[2][8], Vh[2][8];
#define D_RD(i, b, slot) do { LAS char* p_ = (LAS char*)(size_t)(rb[((slot) >> 3) & 1][(i) >> 1] + ((slot) & 7) * D_SLOT + ((i) & 1) * 1024); \
    if constexpr ((i) & 1) Vh[b][(i) >> 1] = tr_read(p_); else Vl[b][(i) >> 1] = tr_read(p_); } while (0)
#define D_STEP(s, NM, LD, RD, FI) do { constexpr int b_ = (s) & 1; \
    if constexpr ((s) == 3 && (FI)) { WAIT_L0(); asm volatile("s_cmp_eq_u32 %0, 0\n\ts_cbranch_scc1 1f\n\ts_barrier\n1:" ::"s"(it) : "scc", "memory"); } \
    else if constexpr (((s) & 3) == 3 && RD) { WAIT_L0(); BAR(); } \
    SCHED0(); \
    static_for<8 * NM>([&](auto I_) { constexpr int i = I_.value, c = i & 7, m = i >> 3; \
      mfma_acc(ac[m][c], cat8(Vl[b_][c], Vh[b_][c]), __builtin_bit_cast(s16x8, Hf[(s) & 7][m])); \
      if constexpr (RD) { if constexpr (NM == 2) D_RD(i, b_ ^ 1, (s) + 1); else { D_RD(2 * i, b_ ^ 1, (s) + 1); D_RD(2 * i + 1, b_ ^ 1, (s) + 1); } } \
      if constexpr (c == 7 && m < LD) H_LD(s, m); \
      SCHED0(); }); \
    ko += 4 * S_ * 16; } while (0)
  f32x4 ac[2][8];
#pragma unroll
  for (int i = 0; i < 16; ++i) { ac[i >> 3][i & 7] = (f32x4){0.f, 0.f, 0.f, 0.f}; OPAQUE(ac[i >> 3][i & 7]); }
  GAS unsigned* fzp = (GAS unsigned*)(ws + WS_FZ + (nb2 * 32 + (lane & 31)) * 128);
  u32x2 zv[8];
  lds_wait(sm + L_CNT, 4);
  STAMP(6);
  static_for<16>([&](auto I_) { D_RD(I_.value, 0, 0); });
  int it;
  for (it = 0; it < N1 / 16 - 1; ++it) static_for<16>([&](auto J_) { D_STEP(J_.value, 2, 2, true, true); });
  unsigned fz = __hip_atomic_load(fzp, RLX);
  static_for<16>([&](auto J_) { constexpr int s = J_.value;
    if constexpr (s == 8) {
      ko = N1 * (4 * S_ * 16);
      wait_flags(fzp, fz, tag);
#pragma unroll
      for (int c = 0; c < 8; ++c) zv[c] = __builtin_amdgcn_raw_buffer_load_b64(rs, WS_Z + ((nb2 * 32 + c * 4 + g) * S_ + row0) * 8, 0, 16);
    }
    D_STEP(s, 2, (s < 8 ? 2 : 1), true, false); });
  MFMA_DONE();
  STAMP(7);
#pragma unroll
  for (int P = 0; P < 4; ++P) {
    u32x4 o;
#pragma unroll
    for (int i = 0; i < 4; ++i) {
      const f32x4 a = ac[1][2 * P + (i >> 1)];
      o[i] = min(pk_bf16(a[(i & 1) * 2], a[(i & 1) * 2 + 1]), 0xfffffffeu);
    }
    ST_SC1(o, WS_EX + pbid * 16384 + P * 4096 + (tid & 255) * 16);
  }
  u32x4 pv[4];
  static_for<N2>([&](auto J_) { constexpr int s = J_.value;
    if constexpr (s == N2 - 8) {
#pragma unroll
      for (int P = 0; P < 4; ++P) xv[P] = *(const u32x4*)(x + (size_t)row0 * D_ + nb2 * 128 + (2 * P + (g & 1)) * 16 + (g >> 1) * 8);
    }
    if constexpr (s == N2 - 3) {
#pragma unroll
      for (int i = 0; i < 4; ++i) pv[i] = LD_SC1(WS_EX + bid * 16384 + i * 4096 + (tid & 255) * 16);
    }
    D_STEP(s, 1, (s + 8 < N2 ? 1 : 0), (s + 1 < N2), false);
  });
  MFMA_DONE();
  STAMP(8);
  for (unsigned spins = 0; spins < SPIN_CAP; ++spins) {
    unsigned mx = 0;
#pragma unroll
    for (int i = 0; i < 16; ++i) mx = max(mx, pv[i >> 2][i & 3]);
    if (!__any(mx == ~0u)) break;
    __builtin_amdgcn_s_sleep(1); FENCE_C();
#pragma unroll
    for (int i = 0; i < 4; ++i) pv[i] = LD_SC1(WS_EX + bid * 16384 + i * 4096 + (tid & 255) * 16);
  }
  STAMP(9);
  {
    const f32x2 sv = *(LAS f32x2*)(sm + L_ST + (row0 & 255) * 8);
    const float ri = __builtin_amdgcn_rcpf(sv[1]), mo = -sv[0] * ri;
    const __amdgpu_buffer_rsrc_t ro = __builtin_amdgcn_make_buffer_rsrc(out, 0, S_ * D_ * 2, 0x00020000);
#pragma unroll
    for (int P = 0; P < 4; ++P) {
      const u32x4 xs = swap16((u32x2){xv[P][0], xv[P][1]}, (u32x2){xv[P][2], xv[P][3]});
      u32x2 o[2];
#pragma unroll
      for (int i = 0; i < 4; ++i) {
        const int c = 2 * P + (i >> 1), d = i & 1;
        const float x0 = bflo(xs[i]), x1 = bfhi(xs[i]);
        const float y0 = fmaf(fmaf(x0, ri, mo), bflo(zv[c][d]), x0) + (ac[0][c][2 * d] + bflo(pv[P][i]));
        const float y1 = fmaf(fmaf(x1, ri, mo), bfhi(zv[c][d]), x1) + (ac[0][c][2 * d + 1] + bfhi(pv[P][i]));
        o[i >> 1][d] = pk_bf16(y0, y1);
      }
      __builtin_amdgcn_raw_buffer_store_b128(swap16(o[0], o[1]), ro, (row0 * D_ + nb2 * 128 + (2 * P + (g & 1)) * 16 + (g >> 1) * 8) * 2, 0, 16);
    }
  }
  STAMP(10);
  WAIT_V0C();
  STAMP(11);
  STAMP_OUT();
}

#define WS_OFF 0
extern "C" void kernel_launch(void* const* d_in, const int* in_sizes, int n_in, void* d_out, int out_size, void* d_ws, size_t ws_size,
                              hipStream_t stream) {
  (void)hipFuncSetAttribute((const void*)k_all, hipFuncAttributeMaxDynamicSharedMemorySize, L_ALLOC);
  k_all<<<256, 512, L_ALLOC, stream>>>((const u16*)d_in[0], (const u16*)d_in[1], (const u16*)d_in[2], (const u16*)d_in[3], (const u16*)d_in[4],
                                     (const u16*)d_in[5], (const u16*)d_in[7], (const u16*)d_in[8], (const u16*)d_in[9], (u16*)d_out, (char*)d_ws + WS_OFF);
}
```

```cpp
#include <hip/hip_runtime.h>
#include <hip/hip_bf16.h>

#define LAS __attribute__((address_space(3)))
#define GAS __attribute__((address_space(1)))
typedef unsigned short u16;
typedef short s16x4 __attribute__((ext_vector_type(4)));
typedef short s16x8 __attribute__((ext_vector_type(8)));
typedef __bf16 bf16x2 __attribute__((ext_vector_type(2)));
typedef __bf16 bf16x8 __attribute__((ext_vector_type(8)));
typedef float f32x2 __attribute__((ext_vector_type(2)));
typedef float f32x4 __attribute__((ext_vector_type(4)));
typedef unsigned u32x4 __attribute__((ext_vector_type(4)));
typedef unsigned u32x2 __attribute__((ext_vector_type(2)));

constexpr int S_ = 2048, D_ = 1024, F_ = 3584;

constexpr unsigned WS_EP = 0;
constexpr unsigned WS_FU = 4096;
constexpr unsigned WS_FH = WS_FU + 32768;
constexpr unsigned WS_FZ = WS_FH + 32768;
constexpr unsigned WS_ST = WS_FZ + 32768;
constexpr unsigned WS_U = 1u << 20;
constexpr unsigned WS_Z = WS_U + S_ * D_ * 2;
constexpr unsigned WS_EX = WS_Z + S_ * D_ * 2;
constexpr unsigned WS_H = WS_EX + 256 * 16384;
constexpr unsigned WS_END = WS_H + S_ * F_ * 2;
static_assert(WS_END <= 0x2000000, "offsets used to drop a store");
constexpr unsigned SPIN_CAP = 1u << 18;

__device__ __forceinline__ float bflo(unsigned v) { return __uint_as_float(v << 16); }
__device__ __forceinline__ float bfhi(unsigned v) { return __uint_as_float(v & 0xffff0000u); }
__device__ __forceinline__ unsigned pk_bf16(float lo, float hi) {
  f32x2 v = {lo, hi};
  return __builtin_bit_cast(unsigned, __builtin_convertvector(v, bf16x2));
}
__device__ __forceinline__ float rbf(float x) { return bflo(pk_bf16(x, x)); }
__device__ __forceinline__ float dot2(unsigned a, unsigned b, float c) {
  return __builtin_amdgcn_fdot2_f32_bf16(__builtin_bit_cast(bf16x2, a), __builtin_bit_cast(bf16x2, b), c, false);
}
template <int CTRL> __device__ __forceinline__ float dpp(float v) {
  return __int_as_float(__builtin_amdgcn_update_dpp(0, __float_as_int(v), CTRL, 0xf, 0xf, true));
}
__device__ __forceinline__ float add32(float a, float b) {
  auto r = __builtin_amdgcn_permlane32_swap(__float_as_uint(a), __float_as_uint(b), false, false);
  return __uint_as_float(r[0]) + __uint_as_float(r[1]);
}
__device__ __forceinline__ float add16(float a, float b) {
  auto r = __builtin_amdgcn_permlane16_swap(__float_as_uint(a), __float_as_uint(b), false, false);
  return __uint_as_float(r[0]) + __uint_as_float(r[1]);
}
__device__ __forceinline__ float sum64(float v) {
  v += dpp<0xB1>(v); v += dpp<0x4E>(v); v += dpp<0x141>(v); v += dpp<0x140>(v);
  v = add16(v, v);
  return add32(v, v);
}
__device__ __forceinline__ u32x4 swap16(u32x2 a, u32x2 b) {
  auto r0 = __builtin_amdgcn_permlane16_swap(a[0], b[0], false, false);
  auto r1 = __builtin_amdgcn_permlane16_swap(a[1], b[1], false, false);
  return (u32x4){r0[0], r1[0], r0[1], r1[1]};
}
__device__ __forceinline__ f32x4 mfma16(s16x8 a, s16x8 b, f32x4 c) {
  return __builtin_amdgcn_mfma_f32_16x16x32_bf16(__builtin_bit_cast(bf16x8, a), __builtin_bit_cast(bf16x8, b), c, 0, 0, 0);
}
__device__ __forceinline__ void mfma_acc(f32x4& c, s16x8 a, s16x8 b) { asm volatile("v_mfma_f32_16x16x32_bf16 %0, %1, %2, %0" : "+v"(c) : "v"(a), "v"(b)); }
#define MFMA_DONE() do { asm volatile("s_nop 7\n\ts_nop 7"); __builtin_amdgcn_sched_barrier(0); } while (0)
__device__ __forceinline__ s16x4 tr_read(LAS char* p) { return __builtin_amdgcn_ds_read_tr16_b64_v4i16((LAS s16x4*)p); }
__device__ __forceinline__ s16x8 cat8(s16x4 a, s16x4 b) { return __builtin_shufflevector(a, b, 0, 1, 2, 3, 4, 5, 6, 7); }
template <int OFF, int DOFF = 0> __device__ __forceinline__ void dma16(const void* base, unsigned voff, unsigned dst) {
  asm volatile("s_add_u32 m0, %0, %4\n\ts_nop 0\n\tglobal_load_lds_dwordx4 %1, %2 offset:%3" ::"s"(dst), "v"(voff), "s"(base), "i"(OFF), "i"(DOFF - OFF) : "scc");
}
template <int OFF, int DOFF> __device__ __forceinline__ void dma16_s(const void* base, unsigned voff, unsigned dst) {
  asm volatile("s_add_u32 m0, %0, %4\n\ts_nop 0\n\tglobal_load_lds_dwordx4 %1, %2 offset:%3" " nt" ::"s"(dst), "v"(voff), "s"(base), "i"(OFF), "i"(DOFF - OFF) : "scc");
}
template <int DOFF, int NL> __device__ __forceinline__ void dma16_m(const void* base, unsigned voff, unsigned dst) {
  asm volatile("s_add_u32 m0, %0, %4\n\ts_bfm_b64 exec, %3, 0\n\tglobal_load_lds_dwordx4 %1, %2\n\ts_mov_b64 exec, -1"
               ::"s"(dst), "v"(voff), "s"(base), "i"(NL), "i"(DOFF) : "scc");
}
template <int NL> __device__ __forceinline__ void touch(const void* base, unsigned v, unsigned dst) {
  unsigned t;
  asm volatile("v_lshlrev_b32 %0, 3, %3\n\ts_mov_b32 m0, %2\n\ts_bfm_b64 exec, %4, 0\n\tglobal_load_lds_dword %0, %1\n\ts_mov_b64 exec, -1"
               : "=&v"(t) : "s"(base), "s"(dst), "v"(v), "i"(NL));
}
template <int L0> __device__ __forceinline__ void touch_hi(const void* base, unsigned v, unsigned dst) {
  asm volatile("s_mov_b32 m0, %1\n\ts_bfm_b64 exec, 4, %3\n\tglobal_load_lds_dword %2, %0\n\ts_mov_b64 exec, -1" :: "s"(base), "s"(dst), "v"(v), "i"(L0));
}
#define LD_HID(d, voff, base, OFF, MOD) asm volatile("global_load_dwordx4 %0, %1, %2 offset:%3" MOD " ; HID %0" : "=&v"(d) : "v"(voff), "s"(base), "i"(OFF))
#define LD_HIDB(d, voff, r, MOD) asm volatile("buffer_load_dwordx4 %0, %1, %2, 0 offen" MOD " ; HID %0" : "=&v"(d) : "v"(voff), "s"(r))
#define LANDED(x) asm volatile("; USE %0" : "+v"(x))
template <int H> __device__ __forceinline__ void colsum(float& cs, s16x8 w, int idx) {
  const u32x4 v = __builtin_bit_cast(u32x4, w);
  unsigned a;
  if constexpr (H == 0)
    asm volatile("s_set_gpr_idx_on %2, gpr_idx(SRC1)\n\tv_lshlrev_b32 %1, 16, %3 ; CSCHK %3 %4\n\ts_set_gpr_idx_off\n\tv_add_f32 %0, %0, %1"
                 : "+v"(cs), "=&v"(a) : "s"(idx), "v"(v[0]), "v"(v));
  else
    asm volatile("s_set_gpr_idx_on %2, gpr_idx(SRC1)\n\tv_and_b32 %1, 0xffff0000, %3 ; CSCHK %3 %4\n\ts_set_gpr_idx_off\n\tv_add_f32 %0, %0, %1"
                 : "+v"(cs), "=&v"(a) : "s"(idx), "v"(v[0]), "v"(v));
}
#define OPAQUE(x) asm volatile("" : "+v"(x))
#define WAIT_V(n) asm volatile("s_waitcnt vmcnt(%0)" ::"i"(n) : "memory")
#define WAIT_L0() __builtin_amdgcn_s_waitcnt(0xC07F)
#define WAIT_V0C() __builtin_amdgcn_s_waitcnt(0x0F70)
#define WAIT_VL(n) do { WAIT_L0(); WAIT_V(n); } while (0)
#define FENCE_C() asm volatile("" ::: "memory")
#define BAR() do { FENCE_C(); __builtin_amdgcn_s_barrier(); FENCE_C(); } while (0)
#define SCHED0() __builtin_amdgcn_sched_barrier(0)
#define RLX __ATOMIC_RELAXED, __HIP_MEMORY_SCOPE_AGENT
#define ST_SC1(v, off) __builtin_amdgcn_raw_buffer_store_b128(v, rs, off, 0, 16)
#define LD_SC1(off) __builtin_amdgcn_raw_buffer_load_b128(rs, off, 0, 16)

template <int V> struct IC { static constexpr int value = V; };
template <int N, int I = 0, class Fn> __device__ __forceinline__ void static_for(Fn&& f) {
  if constexpr (I < N) { f(IC<I>{}); static_for<N, I + 1>(f); }
}

__device__ __forceinline__ void wait_flags(GAS unsigned* p, unsigned v, unsigned tag) {
  if (__builtin_expect(!__all(v == tag), 0)) {
    for (unsigned spins = 0; spins < SPIN_CAP; ++spins) {
      __builtin_amdgcn_s_sleep(2);
      if (__all(__hip_atomic_load(p, RLX) == tag)) break;
    }
  }
  FENCE_C();
}

__device__ __forceinline__ unsigned lds_add(LAS char* p) { return __hip_atomic_fetch_add((LAS unsigned*)p, 1u, __ATOMIC_RELAXED, __HIP_MEMORY_SCOPE_WORKGROUP); }
__device__ __forceinline__ void lds_wait(LAS char* p, unsigned n) {
  FENCE_C();
  for (unsigned spins = 0; __hip_atomic_load((LAS unsigned*)p, __ATOMIC_RELAXED, __HIP_MEMORY_SCOPE_WORKGROUP) != n && spins < SPIN_CAP; ++spins) __builtin_amdgcn_s_sleep(1);
  FENCE_C();
}

constexpr int U_GS = 960;
constexpr int U_UP = 8 * U_GS + 16;
constexpr int U_WS = U_UP + 8 * U_GS;
constexpr int L_A = 61568;
constexpr int L_ST = L_A + 3 * 32768;
constexpr int L_JUNK = L_ST + 2048;
constexpr int L_SK = L_JUNK + 256;
constexpr int L_CNT = L_SK + 1024;
constexpr int L_END = L_CNT + 16;
constexpr int L_ALLOC = L_END;
constexpr int abuf(int u) { return L_A + (u % 3 == 0 ? 0 : u % 3 == 1 ? 65536 : 32768); }
constexpr int CODE_BYTES = 55 * 1024;
constexpr int DM0 = 7, TW = 8, W2T = 11;
constexpr int S_X0 = 1, S_X1 = S_X0 + 1, S_ST = S_X1 + 4, S_KA = 4, S_KB = S_KA + 2, S_KV = S_KB + 4, S_V = S_KV, S_FL = S_V + 10, S_CL = 24;
static_assert(S_ST + 4 <= S_KV || S_ST >= S_V + 7, "side jobs share registers");
constexpr bool w2t(int s) { return s >= W2T && !((s - W2T) & 1) && s < W2T + 14; }
constexpr bool wt(int s) { return s >= 0 && (s & ~1) + TW < 32; }
constexpr int extra(int s) {
  return wt(s) + w2t(s) + 3 * (s == 0) + (s == S_X0 || s == S_X1) + 3 * (s == S_KA) + 2 * (s == S_KB) + (s == S_ST + 3) +
         (s == S_V || s == S_V + 1) + (s == S_CL || s == S_CL + 1) + (s == S_V + 5 || s == S_V + 6) + (s == S_FL);
}
constexpr int L_CSP = 14 * 8192, L_CSF = L_CSP + 4096;
constexpr int C_TS = 32 + 4096, L_T = 143232;
constexpr int L_Z = L_T;
constexpr int C_US = 256 + 4096, L_U = L_T - 4 * C_US;
constexpr int CSPLIT = 20;
static_assert(L_CSP >= abuf(14) && L_T >= abuf(13) && L_T + 4 * C_TS <= L_ST && L_U >= 15 * 8192 && L_T >= 16 * 8192, "lds map");
constexpr int D_SLOT = 8192, N1 = 48, N2 = 16, NT = N1 + N2;
constexpr int PRE = 1, STR = 2, LND = 6, FHP = 29, WGR = 4;
constexpr int staged(int c) { return 2 * PRE + STR * c < 15 ? 2 * PRE + STR * c : 15; }
static_assert(staged(CSPLIT) == 15 && staged(LND) >= 8, "");

template <int LO, int HI, class Hook>
__device__ __forceinline__ void conv_part(LAS char* tp, LAS char* gp, LAS char* zp, bool last, Hook&& hook) {
  constexpr int NH = HI / 2 + 1;
  s16x8 Fh[NH];
#pragma unroll
  for (int n = 0; n < NH; ++n) Fh[n] = cat8(tr_read(tp + n * 256), tr_read(tp + n * 256 - 32));
  WAIT_VL(0); BAR();
  unsigned ga = (unsigned)(size_t)gp, za = (unsigned)(size_t)zp;
  OPAQUE(ga); OPAQUE(za);
  s16x8 G[HI + 1];
#define G_RD(p) G[p] = cat8(tr_read((LAS char*)(size_t)(ga + (p) * 128)), tr_read((LAS char*)(size_t)(ga + (p) * 128 + 32)))
  const f32x4 zero = {0.f, 0.f, 0.f, 0.f};
  f32x4 z[2];
  float ta[4], tb[4], pr[4] = {0.f, 0.f, 0.f, 0.f}, u0, u1;
  auto keep = [&]() {
#pragma unroll
    for (int i = 0; i < 4; ++i) { ta[i] = z[0][i]; tb[i] = z[1][i]; }
  };
  if constexpr (LO == 0) { G_RD(0); G_RD(1); }
  else {
#pragma unroll
    for (int p = 1; p < LO; p += 2) G_RD(p);
    SCHED0();
    static_for<LO / 2>([&](auto J_) { constexpr int j = J_.value;
      z[j & 1] = mfma16(G[2 * j + 1], Fh[LO / 2 - 1 - j], j < 2 ? zero : z[j & 1]);
      G_RD(2 * j);
      SCHED0(); });
    G_RD(LO); G_RD(LO + 1);
    keep();
  }
  auto fin = [&](auto C_, auto K_) { constexpr int c = C_.value, k = K_.value, q = 3 - (k + 1) / 2;
    if constexpr (c < LO) {
      if constexpr (k == 0) { pr[3] = ta[3] + tb[3]; pr[2] = ta[2] + tb[2]; OPAQUE(pr[3]); OPAQUE(pr[2]); }
      if constexpr (k == 2) { pr[2] += dpp<0x13C>(pr[3]); pr[1] = ta[1] + tb[1]; OPAQUE(pr[2]); OPAQUE(pr[1]); }
      if constexpr (k == 4) { pr[1] += dpp<0x13C>(pr[2]); OPAQUE(pr[1]); }
    } else if constexpr (k == 6) {
      const unsigned o = pk_bf16(ta[0], tb[0]);
      *(LAS u16*)(size_t)(za + c * 512) = (u16)o;
      *(LAS u16*)(size_t)(za + c * 512 + 512) = (u16)(o >> 16);
    } else if constexpr (k & 1) {
      ta[q] += dpp<0x13C>(u0); tb[q] += dpp<0x13C>(u1); OPAQUE(ta[q]); OPAQUE(tb[q]);
    } else {
      u0 = last ? pr[q] : ta[q]; u1 = last ? ta[q] : tb[q]; pr[q] = tb[q]; OPAQUE(u0); OPAQUE(u1);
    }
  };
  SCHED0();
  static_for<(HI - LO + 1) / 2>([&](auto C_) {
    constexpr int c = LO + 2 * C_.value, nm = c + 2, g0 = nm > 10 ? 4 : nm > 7 ? nm - 7 : 0;
    hook(IC<c>{});
    static_for<nm>([&](auto J_) { constexpr int j = J_.value, n = c / 2 - j / 2;
      z[j & 1] = mfma16(G[c + (j & 1) - 2 * n], Fh[n], j < 2 ? zero : z[j & 1]);
      if constexpr (j < 2 && c + 2 <= HI) G_RD(c + 2 + j);
      if constexpr (c > 0) static_for<7>([&](auto K_) { if constexpr (j == (g0 + K_.value < nm ? g0 + K_.value : nm - 1)) fin(IC<c - 2>{}, K_); });
      if constexpr (j == nm / 2) hook(IC<c + 1>{});
      SCHED0(); });
    keep();
  });
  static_for<7>([&](auto K_) { fin(IC<HI - 1>{}, K_); });
}

__global__ __launch_bounds__(512, 2) void k_all(const u16* __restrict__ x, const u16* __restrict__ kk, const u16* __restrict__ vv,
                                                const u16* __restrict__ shk, const u16* __restrict__ ssmk, const u16* __restrict__ ssmd,
                                                const u16* __restrict__ w1, const u16* __restrict__ w3, const u16* __restrict__ w2,
                                                u16* __restrict__ out, char* __restrict__ ws) {
  extern __shared__ __attribute__((aligned(16))) char smem[];
  LAS char* sm = (LAS char*)smem;
  int tid = threadIdx.x, lane = tid & 63;
  const int wid = __builtin_amdgcn_readfirstlane(tid >> 6);
  const int bid = blockIdx.x, xcd = bid & 7, rr = bid >> 3;
  const int mb = (xcd & 1) * 4 + (rr & 3), nb = (xcd >> 1) * 8 + (rr >> 2);
  const int wm = wid & 3, wn = wid >> 2;
  int g = lane >> 4, fr = lane & 15, qq = fr >> 2, pp = lane & 3;
  const __amdgpu_buffer_rsrc_t rs = __builtin_amdgcn_make_buffer_rsrc(ws, 0, WS_END, 0x00020000);
  const unsigned lds0 = (unsigned)(size_t)sm;

  const char* gA[4];
#pragma unroll
  for (int i = 0; i < 4; ++i) gA[i] = (const char*)x + (size_t)(mb * 256 + i * 64 + wid * 8) * (D_ * 2);
  const char* gG = (const char*)w1 + ((size_t)(wid * 4) * F_ + nb * 112) * 2;
  const char* gU = (const char*)w3 + ((size_t)(wid * 4) * F_ + nb * 112) * 2;
  const unsigned vA = (lane >> 3) * (D_ * 2) + (((lane & 7) ^ ((wid & 1) * 4 + g)) * 16);
  const int tq = min(wid * 4 + (lane & 3), 27);
  const unsigned vB = lane < 56 ? (lane / 14) * (F_ * 2) + (lane % 14) * 16 : lane < 60 ? (tq / 14) * (F_ * 2) + (tq % 14) * 128 : (wid * 4 + (lane & 3)) * 2048;
  const unsigned wto = (TW * 32 + rr * 2) * (F_ * 2) + (xcd >> 1) * 1792;
  const unsigned lA = lds0 + wid * 1024, lB = lds0 + wid * U_GS;
#define A_DMA(q, i) dma16<((q) >> 1) * 128, abuf((q) >> 1) + (((q) & 1) * 2 + (i)) * 8192>(gA[((q) & 1) * 2 + (i)], vA, lA)
#define W_DMA(slot, i, NL) do { \
    if (i == 0) { dma16_m<(slot) * U_WS, NL>(gG, vB, lB); gG += 32 * F_ * 2; } \
    else { dma16_m<(slot) * U_WS + U_UP, NL>(gU, vB, lB); gU += 32 * F_ * 2; } } while (0)
#define G1_DMA() do { A_DMA(0, 0); A_DMA(0, 1); A_DMA(1, 0); A_DMA(1, 1); W_DMA(0, 0, 56); W_DMA(0, 1, 56); W_DMA(1, 0, 56); W_DMA(1, 1, 56); } while (0)
#define G2_DMA() do { A_DMA(2, 0); A_DMA(2, 1); A_DMA(3, 0); A_DMA(3, 1); W_DMA(2, 0, 56); W_DMA(2, 1, 56); } while (0)
  const int tb = bid >> 4, cb = bid & 15;
  f32x4 acc[4][7];
  u32x4 d[2], kr[5], vr[2];
  unsigned uo[2][4];
  const char* xrow = (const char*)x + (bid * 8 + wid) * (D_ * 2);
  G1_DMA();
#pragma unroll
  for (int i = 0; i < 28; ++i) { acc[i & 3][i >> 2] = (f32x4){0.f, 0.f, 0.f, 0.f}; OPAQUE(acc[i & 3][i >> 2]); }
  if (tid == 0) *(LAS u32x4*)(sm + L_CNT) = (u32x4){0u, 0u, 0u, 0u};
  unsigned ra[2][2], rw[2];
  ra[0][0] = lds0 + L_A + (wm * 64 + fr) * 128 + ((g ^ (fr >> 1)) * 16);
  ra[0][1] = ra[0][0] ^ 64;
  ra[1][0] = ra[0][0] + 65536; ra[1][1] = ra[0][1] + 65536;
  rw[0] = lds0 + 2 * g * U_GS + qq * 224 + wn * 112 + pp * 8;
  rw[1] = lds0 + 2 * g * U_GS + qq * 224 + wn * 112 + 96 + (pp & 1) * 8 + (pp >> 1) * U_UP;
  OPAQUE(ra[0][0]); OPAQUE(ra[0][1]); OPAQUE(ra[1][0]); OPAQUE(ra[1][1]); OPAQUE(rw[0]); OPAQUE(rw[1]);
  SCHED0();
  G2_DMA();
#define U_RD(i, X, Wl, Wh, t) do { \
    if constexpr (i >= 2 && i < 6) X[i - 2] = *(LAS s16x8*)(size_t)(ra[((t) >> 1) % 3 == 1][(t) & 1] + (((t) >> 1) % 3 == 2 ? 32768 : 0) + (i - 2) * 2048); \
    else { constexpr int j_ = i < 2 ? i : i - 4, c_ = j_ >> 1; \
      LAS char* p_ = (LAS char*)(size_t)(rw[c_ == 6] + ((t) & 3) * U_WS + (c_ == 6 ? 0 : (c_ >> 1) * 32 + (c_ & 1) * U_UP) + (j_ & 1) * U_GS); \
      if constexpr (j_ & 1) Wh[c_] = tr_read(p_); else Wl[c_] = tr_read(p_); } } while (0)
  float cs[7] = {0.f, 0.f, 0.f, 0.f, 0.f, 0.f, 0.f};
  s16x8 X[2][4];
  s16x4 Wl[2][7], Wh[2][7];
  WAIT_V(8); BAR();
  static_for<18>([&](auto I_) { constexpr int i = I_.value; U_RD(i, X[0], Wl[0], Wh[0], 0); });
  SCHED0(); WAIT_VL(6); BAR(); SCHED0();

  const int cch = wid & 3;
  unsigned tag = 0, fu = 0, dsk = 0, ep;
  u32x2 skr;
  u32x4 sg;
#define U_DMA(s, i) do { \
    if constexpr ((s) < 28 && (i) < 2) W_DMA((s) & 3, (i), 56); \
    if constexpr ((s) < 27 && (i) >= 2) A_DMA((s) + 5, (i) - 2); \
    if constexpr ((s) == 27 && (i) >= 2) { OPAQUE(tid); const unsigned l_ = tid & 63; \
      dma16_s<((i) - 2) * 1024, ((i) - 2) * 1024 + L_T + 32>((const char*)ssmk + (size_t)(bid * 4 + cch) * 4096 + wn * 2048, l_ * 16, lds0 + cch * C_TS + wn * 2048); } } while (0)
  float ax, ay, bx, by, px, py, qx, qy, mean;
  f32x4 tx, ty;
#define PIN(a, b) do { OPAQUE(a); OPAQUE(b); } while (0)
#define UP2(a, b, v) do { a = bflo(v); b = bfhi(v); PIN(a, b); } while (0)
  auto side = [&](auto S_c, auto I_) { constexpr int s = S_c.value, i = I_.value;
    constexpr bool slot = (i & 3) == 0 || (i & 3) == 3;
    if constexpr (s == 4) {
      if constexpr (i == 0) { LANDED(ep); LANDED(skr); tag = __builtin_amdgcn_readfirstlane(ep) + 0x9E3779B9u; }
      if constexpr (i == 3) UP2(tx[0], tx[1], skr[0]);
      if constexpr (i == 4) UP2(tx[2], tx[3], skr[1]);
      if constexpr (i == 8) { OPAQUE(tid); *(LAS f32x4*)(sm + L_SK + (tid & 63) * 16) = tx; }
    }
    if constexpr (slot && s >= S_ST && s < S_ST + 4) {
      constexpr int n = (s - S_ST) * 14 + (i >> 1), r = n < 22 ? n - 16 : n - 46;
      if constexpr (n == 0) { LANDED(d[0]); LANDED(d[1]); }
      if constexpr (n == 22) PIN(d[0], d[1]);
      if constexpr (n < 16) {
        if constexpr (!(n & 1)) UP2(ax, ay, d[n >> 3][(n >> 1) & 3]);
        else { if constexpr (n == 1) { px = ax; py = ay; } else { px += ax; py += ay; } PIN(px, py); }
      }
      if constexpr (n >= 22 && n < 46) {
        constexpr int j = (n - 22) / 3, k = (n - 22) % 3;
        if constexpr (k == 0) UP2(ax, ay, d[j >> 2][j & 3]);
        if constexpr (k == 1) { ax -= mean; ay -= mean; PIN(ax, ay); }
        if constexpr (k == 2) { px = j ? fmaf(ax, ax, px) : ax * ax; py = j ? fmaf(ay, ay, py) : ay * ay; PIN(px, py); }
      }
      if constexpr (r == 0) { px += py; px += dpp<0xB1>(px); OPAQUE(px); }
      if constexpr (r == 1) { px += dpp<0x4E>(px); OPAQUE(px); }
      if constexpr (r == 2) { px += dpp<0x141>(px); OPAQUE(px); }
      if constexpr (r == 3) { px += dpp<0x140>(px); OPAQUE(px); }
      if constexpr (r == 4) { px = add16(px, px); OPAQUE(px); }
      if constexpr (r == 5) { px = add32(px, px) * (1.0f / D_); OPAQUE(px); if constexpr (n < 22) mean = px; }
      if constexpr (r == 6) { px = rbf(px) + 1e-5f; OPAQUE(px); }
      if constexpr (r == 7) { px = __builtin_amdgcn_sqrtf(rbf(px)); OPAQUE(px); }
      if constexpr (r == 8) { px = rbf(px); mean = rbf(mean); PIN(px, mean); }
      if constexpr (r == 9) {
        OPAQUE(tid);
        ST_SC1(((u32x4){__float_as_uint(mean), tag, __float_as_uint(px), tag}), WS_ST + (bid * 8 + wid) * 16 + (tid & 63) * 0x2000000);
      }
    }
    if constexpr (i == 27 && s >= S_KV - 1 && s < S_KV + 3) {
      OPAQUE(tid);
      tx = *(LAS f32x4*)(sm + L_SK + (tid & 7) * 128 + (s + 1 - S_KV) * 32);
      ty = *(LAS f32x4*)(sm + L_SK + (tid & 7) * 128 + (s + 1 - S_KV) * 32 + 16);
    }
    if constexpr (slot && s >= S_KV && s < S_KV + 4) {
      constexpr int e = s - S_KV, k = i >> 1;
      if constexpr (e == 0 && k == 0) { LANDED(kr[0]); LANDED(kr[1]); LANDED(kr[2]); LANDED(kr[3]); LANDED(kr[4]); }
      if constexpr (k == 0) UP2(ax, ay, kr[4][e]);
      if constexpr (k == 1) UP2(bx, by, kr[3][e]);
      if constexpr (k == 2) { qx = ax * tx[0]; qy = ay * ty[0]; PIN(qx, qy); }
      if constexpr (k == 3) { px = bx * tx[0]; py = by * ty[0]; PIN(px, py); }
      if constexpr (k == 4) UP2(ax, ay, kr[2][e]);
      if constexpr (k == 5) { qx = fmaf(bx, tx[1], qx); qy = fmaf(by, ty[1], qy); PIN(qx, qy); }
      if constexpr (k == 6) { px = fmaf(ax, tx[1], px); py = fmaf(ay, ty[1], py); PIN(px, py); }
      if constexpr (k == 7) UP2(bx, by, kr[1][e]);
      if constexpr (k == 8) { qx = fmaf(ax, tx[2], qx); qy = fmaf(ay, ty[2], qy); PIN(qx, qy); }
      if constexpr (k == 9) { px = fmaf(bx, tx[2], px); py = fmaf(by, ty[2], py); PIN(px, py); }
      if constexpr (k == 10) UP2(ax, ay, kr[0][e]);
      if constexpr (k == 11) { qx = fmaf(bx, tx[3], qx); qy = fmaf(by, ty[3], qy); PIN(qx, qy); }
      if constexpr (k == 12) { px = fmaf(ax, tx[3], px); py = fmaf(ay, ty[3], py); PIN(px, py); }
      if constexpr (k == 13) { uo[0][e] = pk_bf16(px, py); uo[1][e] = pk_bf16(qx, qy); PIN(uo[0][e], uo[1][e]); }
    }
    if constexpr (slot && s >= S_V + 5 && s < S_V + 7) {
      constexpr int e = (s - S_V - 5) * 2 + (i >> 1) / 7, k = (i >> 1) % 7;
      if constexpr (e == 0 && k == 0) { LANDED(vr[0]); LANDED(vr[1]); }
      if constexpr (k == 0) UP2(ax, ay, uo[0][e]);
      if constexpr (k == 1) UP2(px, py, vr[0][e]);
      if constexpr (k == 2) UP2(bx, by, uo[1][e]);
      if constexpr (k == 3) UP2(qx, qy, vr[1][e]);
      if constexpr (k == 4) { px *= ax; py *= ay; PIN(px, py); }
      if constexpr (k == 5) { qx *= bx; qy *= by; PIN(qx, qy); }
      if constexpr (k == 6) { uo[0][e] = pk_bf16(px, py); uo[1][e] = pk_bf16(qx, qy); PIN(uo[0][e], uo[1][e]); }
      if constexpr (i == 27) {
        OPAQUE(tid);
        __builtin_amdgcn_raw_buffer_store_b128((u32x4){uo[0][e - 1], uo[0][e], uo[1][e - 1], uo[1][e]}, rs, (tid & 7) * (2 * S_ * 8) + (tid >> 3) * 16,
                                               WS_U + ((cb * 16 + (e >> 1)) * S_ + tb * 128) * 8, 16);
      }
    }
    if constexpr (i == 20) {
      if constexpr (wt(s)) touch_hi<56>((const char*)(s & 1 ? w3 : w1) + wto + (s & ~1) * 32 * (F_ * 2), vB, lds0 + L_JUNK);
      if constexpr (s == S_CL || s == S_CL + 1) { OPAQUE(tid); __builtin_amdgcn_raw_buffer_store_b128((u32x4){~0u, ~0u, ~0u, ~0u}, rs, tid * 16, WS_EX + bid * 16384 + (s - S_CL) * 8192, 16); }
      if constexpr (s == 0) {
        const char* pc;
        asm volatile("s_getpc_b64 %0" : "=s"(pc));
        touch<2>(pc + min(rr * 8 + wid, CODE_BYTES / 256 - 1) * 256, vB, lds0 + L_JUNK);
        OPAQUE(tid);
        asm volatile("global_load_dwordx2 %0, %1, %2 ; HID %0" : "=&v"(skr) : "v"((tid & 63) * 8), "s"((const char*)shk + cb * 512));
        asm volatile("v_mov_b32 %0, 0\n\tglobal_load_dword %0, %0, %1 sc1 ; HID %0" : "=&v"(ep) : "s"(ws + WS_EP));
      }
      if constexpr (s == S_X0 || s == S_X1) { OPAQUE(tid); LD_HID(d[s - S_X0], (tid & 63) * 16, xrow, (s - S_X0) * 1024, ""); }
      if constexpr (s == S_KA || s == S_KB) {
        OPAQUE(tid);
        const __amdgpu_buffer_rsrc_t rk = __builtin_amdgcn_make_buffer_rsrc((void*)kk, 0, S_ * D_ * 2, 0x00020000);
        const unsigned o = (tid >> 3) * (D_ * 4) + (tid & 7) * 16 + (tb * 128 - 3) * (D_ * 2) + cb * 128;
        if constexpr (s == S_KA) { LD_HIDB(kr[4], o + 4 * D_ * 2, rk, " nt"); LD_HIDB(kr[2], o + 2 * D_ * 2, rk, " nt"); LD_HIDB(kr[0], o, rk, " nt"); }
        else { LD_HIDB(kr[3], o + 3 * D_ * 2, rk, " nt"); LD_HIDB(kr[1], o + D_ * 2, rk, " nt"); }
      }
      if constexpr (s == S_V || s == S_V + 1) {
        OPAQUE(tid);
        LD_HID(vr[s - S_V], (tid >> 3) * (D_ * 4) + (tid & 7) * 16, (const char*)vv + tb * 128 * (D_ * 2) + cb * 128, (s - S_V) * (D_ * 2), " nt");
      }
      if constexpr (s == S_FL) { OPAQUE(tid); __builtin_amdgcn_raw_buffer_store_b32(tag, rs, WS_FU + (cb * 16 + tb) * 128 + min(tid, 1) * 0x2000000, 0, 16); }
    }
    if constexpr (i == 24) {
      if constexpr (w2t(s)) touch_hi<60>((const char*)w2 + ((s - W2T) / 2 * 16 + (bid >> 4)) * (32 * D_ * 2) + (bid & 15) * 128, vB, lds0 + L_JUNK);
    }
  };
  static_for<31>([&](auto S_c) {
    constexpr int s = S_c.value, b = s & 1;
    if constexpr (s == 28) {
      OPAQUE(tid);
      sg = LD_SC1(WS_ST + (mb * 256 + (tid & 255)) * 16);
      fu = __hip_atomic_load((GAS unsigned*)(ws + WS_FU + ((bid >> 4) * 16 + (tid & 15)) * 128), RLX);
      dsk = __hip_atomic_load((GAS u16*)(ssmd + bid * 4 + (tid & 3)), RLX);
    }
    SCHED0();
    static_for<28>([&](auto I_) { constexpr int i = I_.value, c = i >> 2, m = i & 3;
      mfma_acc(acc[m][c], cat8(Wl[b][c], Wh[b][c]), X[b][m]);
      if constexpr (i < 18) U_RD(i, X[b ^ 1], Wl[b ^ 1], Wh[b ^ 1], s + 1);
      if constexpr ((i & 3) == 3 && i >= DM0 && i < DM0 + 16) U_DMA(s, (i - DM0) >> 2);
      if constexpr (s == 0 && i < 6 && i % 3 != 2) { if constexpr (i < 2) W_DMA(3, i, 56); else A_DMA(4, i - 3); }
      side(S_c, I_);
      if constexpr (m == 1 || m == 2) colsum<m - 1>(cs[c], cat8(Wl[b][c], Wh[b][c]), wm);
      if constexpr (s == 30 && i >= 14) colsum<i & 1>(cs[(i - 14) >> 1], cat8(Wl[1][(i - 14) >> 1], Wh[1][(i - 14) >> 1]), wm);
      SCHED0(); });
    if constexpr (s < 30) {
      WAIT_VL((s == 1 ? 10 : s == 28 ? 7 : s == 29 ? 5 : 8) + extra(s - 2) + extra(s - 1) + extra(s));
      BAR(); SCHED0();
    }
  });
  OPAQUE(tid); lane = tid & 63; g = lane >> 4; fr = lane & 15; qq = fr >> 2; pp = lane & 3;
  const int mb2 = mb * 2 + ((rr >> 2) & 1);
  const int kh = xcd >> 2, nb2 = ((xcd >> 1) & 1) * 4 + (rr >> 3), pbid = bid ^ 4;

  const char* wB = (const char*)w2 + ((size_t)(cch * 8) * D_ + nb2 * 128) * 2;
  const char* gB = wB + (size_t)(kh * NT + wn * PRE) * (32 * D_ * 2);
  const unsigned v2 = g * (D_ * 2) + ((fr ^ ((g | ((wid & 1) << 2)) << 1)) * 16);
  const unsigned dB = lds0 + cch * 2048;
#define D_STAGE(d, o) do { dma16<0, (o)>(gB, v2, d); dma16<0, (o) + 1024>(gB + 4 * D_ * 2, v2, d); gB += 32 * D_ * 2; } while (0)
  u32x4 ur[2];
  f32x4 cv[7];
  f32x2 st[4];
  {
    float r[8];
    SCHED0();
    static_for<28>([&](auto I_) { constexpr int i = I_.value, c = i >> 2, m = i & 3;
      mfma_acc(acc[m][c], cat8(Wl[1][c], Wh[1][c]), X[1][m]);
      if constexpr (i == 1) {
        *(LAS float*)(sm + L_CSP + wid * 512 + lane * 4) = add16(add32(cs[0], cs[1]), add32(cs[2], cs[3]));
        *(LAS float*)(sm + L_CSP + wid * 512 + 256 + lane * 4) = add16(add32(cs[4], cs[5]), add32(cs[6], 0.f));
      }
      if constexpr (i == 4) {
        for (unsigned spins = 0; !__all(sg[1] == tag && sg[3] == tag) && spins < SPIN_CAP; ++spins) {
          __builtin_amdgcn_s_sleep(2); FENCE_C();
          sg = LD_SC1(WS_ST + (mb * 256 + (tid & 255)) * 16);
        }
        *(LAS u32x2*)(sm + L_ST + (tid & 255) * 8) = (u32x2){sg[0], sg[2]};
        wait_flags((GAS unsigned*)(ws + WS_FU + ((bid >> 4) * 16 + fr) * 128), fu, tag);
#pragma unroll
        for (int k = 0; k < 2; ++k) ur[k] = __builtin_amdgcn_raw_buffer_load_b128(rs, WS_U + bid * 16384 + tid * 32 + k * 16, 0, 16);
      }
      if constexpr (i == 6) {
        if (wid < 4 && lane == wid) {
          LAS char* p = sm + L_T + wid * C_TS;
          *(LAS u32x4*)p = (u32x4){0u, 0u, 0u, 0u}; *(LAS u32x4*)(p + 16) = (u32x4){0u, 0u, 0u, 0u};
          *(LAS u16*)(p + 32) = (u16)pk_bf16(bflo(*(LAS u16*)(p + 32)) + bflo(dsk), 0.f);
        }
      }
      if constexpr (i == 10) {
        WAIT_L0(); BAR();
#pragma unroll
        for (int k = 0; k < 8; ++k) r[k] = *(LAS float*)(sm + L_CSP + wn * 2048 + k * 256 + lane * 4);
#pragma unroll
        for (int k = 0; k < 4; ++k) st[k] = *(LAS f32x2*)(sm + L_ST + (wm * 64 + k * 16 + fr) * 8);
      }
      if constexpr (i == 16) {
        LAS char* q = sm + L_CSF + wid * 512;
        *(LAS float*)(q + (((g & 1) << 1) | (g >> 1)) * 64 + fr * 4) = (r[0] + r[2]) + (r[4] + r[6]);
        *(LAS float*)(q + (((g & 1) << 1) | (g >> 1)) * 64 + 256 + fr * 4) = (r[1] + r[3]) + (r[5] + r[7]);
#pragma unroll
        for (int c2 = 0; c2 < 7; ++c2) cv[c2] = *(LAS f32x4*)(q + c2 * 64 + g * 16);
      }
      if constexpr (i == 23) D_STAGE(dB + wn * (PRE * D_SLOT), 0);
      if constexpr (i == 20) {
        if (tid < 64) *(LAS u32x4*)(sm + L_U + g * C_US + fr * 16) = (u32x4){0u, 0u, 0u, 0u};
      }
      SCHED0(); });
  }
  MFMA_DONE();

  {
    float ea[4], s2[4];
#pragma unroll
    for (int m = 0; m < 4; ++m) { ea[m] = -1.44269504089f * __builtin_amdgcn_rcpf(st[m][1]); s2[m] = st[m][1] * st[m][1]; }
    auto sw = [](f32x2 a, f32x2 b, float e, float q) {
      const f32x2 t = a * e;
      const f32x2 d = (f32x2){__builtin_amdgcn_exp2f(t[0]), __builtin_amdgcn_exp2f(t[1])} * q + q;
      const f32x2 h = a * b * (f32x2){__builtin_amdgcn_rcpf(d[0]), __builtin_amdgcn_rcpf(d[1])};
      return pk_bf16(h[0], h[1]);
    };
    const unsigned hb = WS_H + ((nb * 14 + wn * 7) * S_ + mb * 256 + wm * 64) * 16;
    const unsigned ho = ((g >> 1) * S_ + (g & 1) * 16 + fr) * 16;
#pragma unroll
    for (int P = 0; P < 3; ++P)
#pragma unroll
      for (int mp = 0; mp < 2; ++mp) {
        u32x2 h[2];
#pragma unroll
        for (int i = 0; i < 2; ++i) {
          const int m = 2 * mp + i;
          const f32x4 ga = acc[m][2 * P] - st[m][0] * cv[2 * P], up = acc[m][2 * P + 1] - st[m][0] * cv[2 * P + 1];
          h[i] = (u32x2){sw(ga.xy, up.xy, ea[m], s2[m]), sw(ga.zw, up.zw, ea[m], s2[m])};
        }
        ST_SC1(swap16(h[0], h[1]), hb + ho + (2 * P * S_ + mp * 32) * 16);
        SCHED0();
      }
    u32x2 t6[2];
#pragma unroll
    for (int mp = 0; mp < 2; ++mp) {
      const float e = lane < 32 ? ea[2 * mp] : ea[2 * mp + 1], q = lane < 32 ? s2[2 * mp] : s2[2 * mp + 1];
      f32x4 ga = acc[2 * mp][6] - st[2 * mp][0] * cv[6], up = acc[2 * mp + 1][6] - st[2 * mp + 1][0] * cv[6];
#pragma unroll
      for (int j = 0; j < 4; ++j) {
        auto r = __builtin_amdgcn_permlane32_swap(__float_as_uint(ga[j]), __float_as_uint(up[j]), false, false);
        ga[j] = __uint_as_float(r[0]); up[j] = __uint_as_float(r[1]);
      }
      t6[mp] = (u32x2){sw(ga.xy, up.xy, e, q), sw(ga.zw, up.zw, e, q)};
    }
    ST_SC1(swap16(t6[0], t6[1]), hb + (6 * S_ + ((g & 1) * 2 + (g >> 1)) * 16 + fr) * 16);
  }
#pragma unroll
  for (int c = 0; c < 4; ++c) {
    const unsigned sel = (c & 1) ? 0x07060302u : 0x05040100u;
    *(LAS u32x2*)(sm + L_U + c * C_US + 256 + tid * 8) =
        (u32x2){__builtin_amdgcn_perm(ur[0][2 + (c >> 1)], ur[0][c >> 1], sel), __builtin_amdgcn_perm(ur[1][2 + (c >> 1)], ur[1][c >> 1], sel)};
  }

  LAS char* tp = sm + L_T + cch * C_TS + 32 + (-12 + 4 * pp + 4 * (31 - 8 * g - qq)) * 2;
  LAS char* gp = sm + L_U + cch * C_US + 256 + (16 * pp - 112 + 4 * (8 * g + qq)) * 2;
  LAS char* zp = sm + L_Z + lane * 8 + cch * 2;
  if (wid < 4) {
    gB = wB + (size_t)(kh * NT + 2 * PRE) * (32 * D_ * 2);
#define D_GROUP(j) do { const unsigned d_ = dB + (((j) + 3) & 3) * (4 * D_SLOT);     \
      D_STAGE(dB + (((j) + 2) & 3) * (4 * D_SLOT), 3 * D_SLOT); \
      if ((j) + 3 == N1 / 4) gB = wB + (size_t)N1 * (32 * D_ * 2); \
      D_STAGE(d_, 0); D_STAGE(d_, D_SLOT); D_STAGE(d_, 2 * D_SLOT); } while (0)
    conv_part<0, CSPLIT - 1>(tp, gp, zp, lane == 63, [&](auto P_) { constexpr int p = P_.value;
      if constexpr (p == 0) __builtin_amdgcn_raw_buffer_store_b32(tag, rs, WS_FH + (mb * 32 + nb) * 128 + min(tid, 1) * 0x2000000, 0, 16);
      if constexpr (p == LND) { WAIT_V(2 * (staged(p) - 8)); if (lane == 0) lds_add(sm + L_CNT); }
      static_for<staged(p + 1) - staged(p)>([&](auto I_) { D_STAGE(dB, (staged(p) + I_.value) * D_SLOT); }); });
    FENCE_C();
    if (lane == 0) lds_add(sm + L_CNT + 8);
    lds_wait(sm + L_CNT + 8, 8);
    D_STAGE(dB, 15 * D_SLOT);
    {
      u32x4 o[4];
#pragma unroll
      for (int i = 0; i < 4; ++i) o[i] = *(LAS u32x4*)(sm + L_Z + i * 4096 + tid * 16);
#pragma unroll
      for (int i = 0; i < 4; ++i) ST_SC1(o[i], WS_Z + bid * 16384 + i * 4096 + tid * 16);
    }
    WAIT_V(6); BAR();
    static_for<7>([&](auto I_) { D_STAGE(dB, I_.value * D_SLOT); });
    WAIT_V(18); BAR();
    D_GROUP(3);
    for (int j = 4; j < NT / 4 - 3; ++j) {
      WAIT_V(14); BAR();
      if (j == 4 && tid == 0) __hip_atomic_store((GAS unsigned*)(ws + WS_FZ + bid * 128), tag, RLX);
      D_GROUP(j);
    }
    WAIT_V(14); BAR();
    D_STAGE(dB, ((NT - 1) & 15) * D_SLOT);
    WAIT_V(8); BAR();
    WAIT_V(0); BAR();
    if (bid == 0 && wid == 3) {
#pragma unroll
      for (int i = 0; i < 4; ++i) { GAS unsigned* fp = (GAS unsigned*)(ws + WS_FU + (i * 64 + lane) * 128); wait_flags(fp, __hip_atomic_load(fp, RLX), tag); }
      if (lane == 0) __hip_atomic_store((GAS unsigned*)(ws + WS_EP), tag, RLX);
    }
    return;
  }

  __builtin_amdgcn_s_setprio(1);
  unsigned fh;
  conv_part<CSPLIT, 31>(tp, gp, zp, lane == 63, [&](auto P_) {
    if constexpr (P_.value == FHP) {
      OPAQUE(tid); const int l = tid & 63;
      fh = __builtin_amdgcn_raw_buffer_load_b32(rs, (l < 19 ? kh * 13 + l : nb ^ 16) * 128, WS_FH + mb * 32 * 128, 16);
    } });
  FENCE_C();
  if (lane == 0) lds_add(sm + L_CNT + 8);
  const int row0 = mb2 * 128 + kh * 64 + cch * 16 + fr;
  u32x4 xv[4];
  {
    GAS unsigned* fp = (GAS unsigned*)(ws + (lane < 19 ? WS_FH + (mb * 32 + kh * 13 + lane) * 128 : WS_FH + (mb * 32 + (nb ^ 16)) * 128));
    wait_flags(fp, fh, tag);
  }
  const unsigned vK = WS_H + (g * S_ + row0) * 16, vS = vK ^ 1024;
  unsigned ko = kh * NT * (4 * S_ * 16);
  u32x4 Hf[8][2];
#define H_LD(t, m) Hf[(t) & 7][m] = __builtin_amdgcn_raw_buffer_load_b128(rs, (m) ? vS : vK, ko, 16)
  static_for<8>([&](auto I_) { H_LD(I_.value, 0); H_LD(I_.value, 1); ko += 4 * S_ * 16; });
  unsigned rb[2][8];
#pragma unroll
  for (int c = 0; c < 8; ++c) {
    rb[0][c] = lds0 + (8 * g + qq) * 256 + ((c ^ (qq | ((g & 1) << 2))) * 32) + pp * 8;
    rb[1][c] = rb[0][c] + 8 * D_SLOT; OPAQUE(rb[0][c]); OPAQUE(rb[1][c]);
  }
  s16x4 Vl[2][8], Vh[2][8];
#define D_RD(i, b, slot) do { LAS char* p_ = (LAS char*)(size_t)(rb[((slot) >> 3) & 1][(i) >> 1] + ((slot) & 7) * D_SLOT + ((i) & 1) * 1024); \
    if constexpr ((i) & 1) Vh[b][(i) >> 1] = tr_read(p_); else Vl[b][(i) >> 1] = tr_read(p_); } while (0)
#define D_STEP(s, NM, LD, RD, FI) do { constexpr int b_ = (s) & 1; \
    if constexpr ((s) == 3 && (FI)) { asm volatile("s_cmp_eq_u32 %0, 0\n\ts_cbranch_scc1 1f\n\ts_barrier\n1:" ::"s"(it) : "scc", "memory"); } \
    else if constexpr (((s) & 3) == 3 && RD) BAR(); \
    SCHED0(); \
    static_for<8 * NM>([&](auto I_) { constexpr int i = I_.value, c = NM == 2 ? i >> 1 : i, m = NM == 2 ? i & 1 : 0; \
      if constexpr (c % WGR == 0 && m == 0 && RD) __builtin_amdgcn_s_waitcnt(0xC07F | (16 - 2 * WGR) << 8); \
      mfma_acc(ac[m][c], cat8(Vl[b_][c], Vh[b_][c]), __builtin_bit_cast(s16x8, Hf[(s) & 7][m])); \
      if constexpr (RD) { if constexpr (NM == 2) D_RD(i, b_ ^ 1, (s) + 1); else { D_RD(2 * i, b_ ^ 1, (s) + 1); D_RD(2 * i + 1, b_ ^ 1, (s) + 1); } } \
      if constexpr (c == 7 && m < LD) H_LD(s, m); \
      SCHED0(); }); \
    ko += 4 * S_ * 16; } while (0)
  f32x4 ac[2][8];
#pragma unroll
  for (int i = 0; i < 16; ++i) { ac[i >> 3][i & 7] = (f32x4){0.f, 0.f, 0.f, 0.f}; OPAQUE(ac[i >> 3][i & 7]); }
  GAS unsigned* fzp = (GAS unsigned*)(ws + WS_FZ + (nb2 * 32 + (lane & 31)) * 128);
  u32x2 zv[8];
  lds_wait(sm + L_CNT, 4);
  static_for<16>([&](auto I_) { D_RD(I_.value, 0, 0); });
  int it;
  for (it = 0; it < N1 / 16 - 1; ++it) static_for<16>([&](auto J_) { D_STEP(J_.value, 2, 2, true, true); });
  unsigned fz = __hip_atomic_load(fzp, RLX);
  static_for<16>([&](auto J_) { constexpr int s = J_.value;
    if constexpr (s == 8) {
      ko = N1 * (4 * S_ * 16);
      wait_flags(fzp, fz, tag);
#pragma unroll
      for (int c = 0; c < 8; ++c) zv[c] = __builtin_amdgcn_raw_buffer_load_b64(rs, WS_Z + ((nb2 * 32 + c * 4 + g) * S_ + row0) * 8, 0, 16);
    }
    D_STEP(s, 2, (s < 8 ? 2 : 1), true, false); });
  MFMA_DONE();
#pragma unroll
  for (int P = 0; P < 4; ++P) {
    u32x4 o;
#pragma unroll
    for (int i = 0; i < 4; ++i) {
      const f32x4 a = ac[1][2 * P + (i >> 1)];
      o[i] = min(pk_bf16(a[(i & 1) * 2], a[(i & 1) * 2 + 1]), 0xfffffffeu);
    }
    ST_SC1(o, WS_EX + pbid * 16384 + P * 4096 + (tid & 255) * 16);
  }
  u32x4 pv[4];
  static_for<N2>([&](auto J_) { constexpr int s = J_.value;
    if constexpr (s == N2 - 8) {
#pragma unroll
      for (int P = 0; P < 4; ++P) xv[P] = *(const u32x4*)(x + (size_t)row0 * D_ + nb2 * 128 + (2 * P + (g & 1)) * 16 + (g >> 1) * 8);
    }
    if constexpr (s == N2 - 3) {
#pragma unroll
      for (int i = 0; i < 4; ++i) pv[i] = LD_SC1(WS_EX + bid * 16384 + i * 4096 + (tid & 255) * 16);
    }
    D_STEP(s, 1, (s + 8 < N2 ? 1 : 0), (s + 1 < N2), false);
  });
  MFMA_DONE();
  for (unsigned spins = 0; spins < SPIN_CAP; ++spins) {
    unsigned mx = 0;
#pragma unroll
    for (int i = 0; i < 16; ++i) mx = max(mx, pv[i >> 2][i & 3]);
    if (!__any(mx == ~0u)) break;
    __builtin_amdgcn_s_sleep(1); FENCE_C();
#pragma unroll
    for (int i = 0; i < 4; ++i) pv[i] = LD_SC1(WS_EX + bid * 16384 + i * 4096 + (tid & 255) * 16);
  }
  {
    const f32x2 sv = *(LAS f32x2*)(sm + L_ST + (row0 & 255) * 8);
    const float ri = __builtin_amdgcn_rcpf(sv[1]), mo = -sv[0] * ri;
    const __amdgpu_buffer_rsrc_t ro = __builtin_amdgcn_make_buffer_rsrc(out, 0, S_ * D_ * 2, 0x00020000);
#pragma unroll
    for (int P = 0; P < 4; ++P) {
      const u32x4 xs = swap16((u32x2){xv[P][0], xv[P][1]}, (u32x2){xv[P][2], xv[P][3]});
      u32x2 o[2];
#pragma unroll
      for (int i = 0; i < 4; ++i) {
        const int c = 2 * P + (i >> 1), d = i & 1;
        const float x0 = bflo(xs[i]), x1 = bfhi(xs[i]);
        const float y0 = fmaf(fmaf(x0, ri, mo), bflo(zv[c][d]), x0) + (ac[0][c][2 * d] + bflo(pv[P][i]));
        const float y1 = fmaf(fmaf(x1, ri, mo), bfhi(zv[c][d]), x1) + (ac[0][c][2 * d + 1] + bfhi(pv[P][i]));
        o[i >> 1][d] = pk_bf16(y0, y1);
      }
      __builtin_amdgcn_raw_buffer_store_b128(swap16(o[0], o[1]), ro, (row0 * D_ + nb2 * 128 + (2 * P + (g & 1)) * 16 + (g >> 1) * 8) * 2, 0, 16);
    }
  }
  WAIT_V0C();
}

extern "C" void kernel_launch(void* const* d_in, const int* in_sizes, int n_in, void* d_out, int out_size, void* d_ws, size_t ws_size,
                              hipStream_t stream) {
  (void)hipFuncSetAttribute((const void*)k_all, hipFuncAttributeMaxDynamicSharedMemorySize, L_ALLOC);
  k_all<<<256, 512, L_ALLOC, stream>>>((const u16*)d_in[0], (const u16*)d_in[1], (const u16*)d_in[2], (const u16*)d_in[3], (const u16*)d_in[4],
                                     (const u16*)d_in[5], (const u16*)d_in[7], (const u16*)d_in[8], (const u16*)d_in[9], (u16*)d_out, (char*)d_ws);
}
```

```cpp
#include <hip/hip_runtime.h>
#include <hip/hip_bf16.h>

#define LAS __attribute__((address_space(3)))
#define GAS __attribute__((address_space(1)))
typedef unsigned short u16;
typedef short s16x4 __attribute__((ext_vector_type(4)));
typedef short s16x8 __attribute__((ext_vector_type(8)));
typedef __bf16 bf16x2 __attribute__((ext_vector_type(2)));
typedef __bf16 bf16x8 __attribute__((ext_vector_type(8)));
typedef float f32x2 __attribute__((ext_vector_type(2)));
typedef float f32x4 __attribute__((ext_vector_type(4)));
typedef unsigned u32x4 __attribute__((ext_vector_type(4)));
typedef unsigned u32x2 __attribute__((ext_vector_type(2)));

constexpr int S_ = 2048, D_ = 1024, F_ = 3584;

constexpr unsigned WS_EP = 0;
constexpr unsigned WS_FU = 4096;
constexpr unsigned WS_FH = WS_FU + 32768;
constexpr unsigned WS_FZ = WS_FH + 32768;
constexpr unsigned WS_ST = WS_FZ + 32768;
constexpr unsigned WS_CS = WS_ST + 32768;
constexpr unsigned WS_U = 1u << 20;
constexpr unsigned WS_Z = WS_U + S_ * D_ * 2;
constexpr unsigned WS_EX = WS_Z + S_ * D_ * 2;
constexpr unsigned WS_H = WS_EX + 256 * 16384;
constexpr unsigned WS_END = WS_H + S_ * F_ * 2;
static_assert(WS_END <= 0x2000000, "offsets used to drop a store");
constexpr unsigned SPIN_CAP = 1u << 18;

__device__ __forceinline__ float bflo(unsigned v) { return __uint_as_float(v << 16); }
__device__ __forceinline__ float bfhi(unsigned v) { return __uint_as_float(v & 0xffff0000u); }
__device__ __forceinline__ unsigned pk_bf16(float lo, float hi) {
  f32x2 v = {lo, hi};
  return __builtin_bit_cast(unsigned, __builtin_convertvector(v, bf16x2));
}
__device__ __forceinline__ float rbf(float x) { return bflo(pk_bf16(x, x)); }
__device__ __forceinline__ float dot2(unsigned a, unsigned b, float c) {
  return __builtin_amdgcn_fdot2_f32_bf16(__builtin_bit_cast(bf16x2, a), __builtin_bit_cast(bf16x2, b), c, false);
}
template <int CTRL> __device__ __forceinline__ float dpp(float v) {
  return __int_as_float(__builtin_amdgcn_update_dpp(0, __float_as_int(v), CTRL, 0xf, 0xf, true));
}
__device__ __forceinline__ float add32(float a, float b) {
  auto r = __builtin_amdgcn_permlane32_swap(__float_as_uint(a), __float_as_uint(b), false, false);
  return __uint_as_float(r[0]) + __uint_as_float(r[1]);
}
__device__ __forceinline__ float add16(float a, float b) {
  auto r = __builtin_amdgcn_permlane16_swap(__float_as_uint(a), __float_as_uint(b), false, false);
  return __uint_as_float(r[0]) + __uint_as_float(r[1]);
}
__device__ __forceinline__ float sum64(float v) {
  v += dpp<0xB1>(v); v += dpp<0x4E>(v); v += dpp<0x141>(v); v += dpp<0x140>(v);
  v = add16(v, v);
  return add32(v, v);
}
__device__ __forceinline__ u32x4 swap16(u32x2 a, u32x2 b) {
  auto r0 = __builtin_amdgcn_permlane16_swap(a[0], b[0], false, false);
  auto r1 = __builtin_amdgcn_permlane16_swap(a[1], b[1], false, false);
  return (u32x4){r0[0], r1[0], r0[1], r1[1]};
}
__device__ __forceinline__ f32x4 mfma16(s16x8 a, s16x8 b, f32x4 c) {
  return __builtin_amdgcn_mfma_f32_16x16x32_bf16(__builtin_bit_cast(bf16x8, a), __builtin_bit_cast(bf16x8, b), c, 0, 0, 0);
}
__device__ __forceinline__ void mfma_acc(f32x4& c, s16x8 a, s16x8 b) { asm volatile("v_mfma_f32_16x16x32_bf16 %0, %1, %2, %0" : "+v"(c) : "v"(a), "v"(b)); }
#define MFMA_DONE() do { asm volatile("s_nop 7\n\ts_nop 7"); __builtin_amdgcn_sched_barrier(0); } while (0)
__device__ __forceinline__ s16x4 tr_read(LAS char* p) { return __builtin_amdgcn_ds_read_tr16_b64_v4i16((LAS s16x4*)p); }
__device__ __forceinline__ s16x8 cat8(s16x4 a, s16x4 b) { return __builtin_shufflevector(a, b, 0, 1, 2, 3, 4, 5, 6, 7); }
template <int OFF, int DOFF = 0> __device__ __forceinline__ void dma16(const void* base, unsigned voff, unsigned dst) {
  asm volatile("s_add_u32 m0, %0, %4\n\ts_nop 0\n\tglobal_load_lds_dwordx4 %1, %2 offset:%3" ::"s"(dst), "v"(voff), "s"(base), "i"(OFF), "i"(DOFF - OFF) : "scc");
}
template <int OFF, int DOFF> __device__ __forceinline__ void dma16_s(const void* base, unsigned voff, unsigned dst) {
  asm volatile("s_add_u32 m0, %0, %4\n\ts_nop 0\n\tglobal_load_lds_dwordx4 %1, %2 offset:%3" " nt" ::"s"(dst), "v"(voff), "s"(base), "i"(OFF), "i"(DOFF - OFF) : "scc");
}
template <int DOFF, int NL> __device__ __forceinline__ void dma16_m(const void* base, unsigned voff, unsigned dst) {
  asm volatile("s_add_u32 m0, %0, %4\n\ts_bfm_b64 exec, %3, 0\n\tglobal_load_lds_dwordx4 %1, %2\n\ts_mov_b64 exec, -1"
               ::"s"(dst), "v"(voff), "s"(base), "i"(NL), "i"(DOFF) : "scc");
}
template <int NL> __device__ __forceinline__ void touch(const void* base, unsigned v, unsigned dst) {
  unsigned t;
  asm volatile("v_lshlrev_b32 %0, 3, %3\n\ts_mov_b32 m0, %2\n\ts_bfm_b64 exec, %4, 0\n\tglobal_load_lds_dword %0, %1\n\ts_mov_b64 exec, -1"
               : "=&v"(t) : "s"(base), "s"(dst), "v"(v), "i"(NL));
}
template <int L0> __device__ __forceinline__ void touch_hi(const void* base, unsigned v, unsigned dst) {
  asm volatile("s_mov_b32 m0, %1\n\ts_bfm_b64 exec, 4, %3\n\tglobal_load_lds_dword %2, %0\n\ts_mov_b64 exec, -1" :: "s"(base), "s"(dst), "v"(v), "i"(L0));
}
#define LD_HID(d, voff, base, OFF, MOD) asm volatile("global_load_dwordx4 %0, %1, %2 offset:%3" MOD " ; HID %0" : "=&v"(d) : "v"(voff), "s"(base), "i"(OFF))
#define LD_HIDB(d, voff, r, MOD) asm volatile("buffer_load_dwordx4 %0, %1, %2, 0 offen" MOD " ; HID %0" : "=&v"(d) : "v"(voff), "s"(r))
#define LANDED(x) asm volatile("; USE %0" : "+v"(x))
template <int H> __device__ __forceinline__ void colsum(float& cs, s16x8 w, int idx) {
  const u32x4 v = __builtin_bit_cast(u32x4, w);
  unsigned a;
  if constexpr (H == 0)
    asm volatile("s_set_gpr_idx_on %2, gpr_idx(SRC1)\n\tv_lshlrev_b32 %1, 16, %3 ; CSCHK %3 %4\n\ts_set_gpr_idx_off\n\tv_add_f32 %0, %0, %1"
                 : "+v"(cs), "=&v"(a) : "s"(idx), "v"(v[0]), "v"(v));
  else
    asm volatile("s_set_gpr_idx_on %2, gpr_idx(SRC1)\n\tv_and_b32 %1, 0xffff0000, %3 ; CSCHK %3 %4\n\ts_set_gpr_idx_off\n\tv_add_f32 %0, %0, %1"
                 : "+v"(cs), "=&v"(a) : "s"(idx), "v"(v[0]), "v"(v));
}
#define OPAQUE(x) asm volatile("" : "+v"(x))
#define WAIT_V(n) asm volatile("s_waitcnt vmcnt(%0)" ::"i"(n) : "memory")
#define WAIT_L0() __builtin_amdgcn_s_waitcnt(0xC07F)
#define WAIT_V0C() __builtin_amdgcn_s_waitcnt(0x0F70)
#define WAIT_VL(n) do { WAIT_L0(); WAIT_V(n); } while (0)
#define FENCE_C() asm volatile("" ::: "memory")
#define BAR() do { FENCE_C(); __builtin_amdgcn_s_barrier(); FENCE_C(); } while (0)
#define SCHED0() __builtin_amdgcn_sched_barrier(0)
#define RLX __ATOMIC_RELAXED, __HIP_MEMORY_SCOPE_AGENT
#define ST_SC1(v, off) __builtin_amdgcn_raw_buffer_store_b128(v, rs, off, 0, 16)
#define LD_SC1(off) __builtin_amdgcn_raw_buffer_load_b128(rs, off, 0, 16)

template <int V> struct IC { static constexpr int value = V; };
template <int N, int I = 0, class Fn> __device__ __forceinline__ void static_for(Fn&& f) {
  if constexpr (I < N) { f(IC<I>{}); static_for<N, I + 1>(f); }
}

__device__ __forceinline__ void wait_flags(GAS unsigned* p, unsigned v, unsigned tag) {
  if (__builtin_expect(!__all(v == tag), 0)) {
    for (unsigned spins = 0; spins < SPIN_CAP; ++spins) {
      __builtin_amdgcn_s_sleep(2);
      if (__all(__hip_atomic_load(p, RLX) == tag)) break;
    }
  }
  FENCE_C();
}

__device__ __forceinline__ unsigned lds_add(LAS char* p) { return __hip_atomic_fetch_add((LAS unsigned*)p, 1u, __ATOMIC_RELAXED, __HIP_MEMORY_SCOPE_WORKGROUP); }
__device__ __forceinline__ void lds_wait(LAS char* p, unsigned n) {
  FENCE_C();
  for (unsigned spins = 0; __hip_atomic_load((LAS unsigned*)p, __ATOMIC_RELAXED, __HIP_MEMORY_SCOPE_WORKGROUP) != n && spins < SPIN_CAP; ++spins) __builtin_amdgcn_s_sleep(1);
  FENCE_C();
}

constexpr int U_GS = 960;
constexpr int U_UP = 8 * U_GS + 16;
constexpr int U_WS = U_UP + 8 * U_GS;
constexpr int L_A = 61568;
constexpr int L_ST = L_A + 3 * 32768;
constexpr int L_JUNK = L_ST + 2048;
constexpr int L_SK = L_JUNK + 256;
constexpr int L_CNT = L_SK + 1024;
constexpr int L_END = L_CNT + 16;
constexpr int L_ALLOC = L_END;
constexpr int abuf(int u) { return L_A + (u % 3 == 0 ? 0 : u % 3 == 1 ? 65536 : 32768); }
constexpr int CODE_BYTES = 50 * 1024;
constexpr int DM0 = 7, TW = 8, W2T = 11, CS0 = 23;
constexpr int S_X0 = 1, S_X1 = S_X0 + 1, S_ST = S_X1 + 4, S_KA = 4, S_KB = S_KA + 2, S_KV = S_KB + 4, S_V = S_KV, S_FL = S_V + 10, S_CL = 24;
static_assert(S_ST + 4 <= S_KV || S_ST >= S_V + 7, "side jobs share registers");
constexpr bool w2t(int s) { return s >= W2T && !((s - W2T) & 1) && s < W2T + 14; }
constexpr bool wt(int s) { return s >= 0 && (s & ~1) + TW < 32; }
constexpr int extra(int s) {
  return wt(s) + w2t(s) + 3 * (s == 0) + (s == S_X0 || s == S_X1) + 3 * (s == S_KA) + 2 * (s == S_KB) + (s == S_ST + 3) +
         (s == S_V || s == S_V + 1) + (s == S_CL || s == S_CL + 1) + (s == S_V + 5 || s == S_V + 6) + (s == S_FL) + (s == CS0 - 1);
}
constexpr int L_CSG = 13 * 8192, L_CSP = 14 * 8192, L_CSF = L_CSP + 4096;
constexpr int C_TS = 32 + 4096, L_T = 143232;
constexpr int L_Z = L_T;
constexpr int C_US = 256 + 4096, L_U = L_T - 4 * C_US;
constexpr int CSPLIT = 20;
static_assert(L_CSG >= abuf(14) && L_T >= abuf(13) && L_T + 4 * C_TS <= L_ST && L_U >= 15 * 8192 && L_T >= 16 * 8192, "lds map");
constexpr int D_SLOT = 8192, N1 = 48, N2 = 16, NT = N1 + N2;
constexpr int PRE = 1, STR = 2, LND = 6, FHP = 29, WGR = 4;
constexpr int staged(int c) { return 2 * PRE + STR * c < 15 ? 2 * PRE + STR * c : 15; }
static_assert(staged(CSPLIT) == 15 && staged(LND) >= 8, "");

template <int LO, int HI, class Hook>
__device__ __forceinline__ void conv_part(LAS char* tp, LAS char* gp, LAS char* zp, bool last, Hook&& hook) {
  constexpr int NH = HI / 2 + 1;
  s16x8 Fh[NH];
#pragma unroll
  for (int n = 0; n < NH; ++n) Fh[n] = cat8(tr_read(tp + n * 256), tr_read(tp + n * 256 - 32));
  WAIT_VL(0); BAR();
  unsigned ga = (unsigned)(size_t)gp, za = (unsigned)(size_t)zp;
  OPAQUE(ga); OPAQUE(za);
  s16x8 G[HI + 1];
#define G_RD(p) G[p] = cat8(tr_read((LAS char*)(size_t)(ga + (p) * 128)), tr_read((LAS char*)(size_t)(ga + (p) * 128 + 32)))
  const f32x4 zero = {0.f, 0.f, 0.f, 0.f};
  f32x4 z[2];
  float ta[4], tb[4], pr[4] = {0.f, 0.f, 0.f, 0.f}, u0, u1;
  auto keep = [&]() {
#pragma unroll
    for (int i = 0; i < 4; ++i) { ta[i] = z[0][i]; tb[i] = z[1][i]; }
  };
  if constexpr (LO == 0) { G_RD(0); G_RD(1); }
  else {
#pragma unroll
    for (int p = 1; p < LO; p += 2) G_RD(p);
    SCHED0();
    static_for<LO / 2>([&](auto J_) { constexpr int j = J_.value;
      z[j & 1] = mfma16(G[2 * j + 1], Fh[LO / 2 - 1 - j], j < 2 ? zero : z[j & 1]);
      G_RD(2 * j);
      SCHED0(); });
    G_RD(LO); G_RD(LO + 1);
    keep();
  }
  auto fin = [&](auto C_, auto K_) { constexpr int c = C_.value, k = K_.value, q = 3 - (k + 1) / 2;
    if constexpr (c < LO) {
      if constexpr (k == 0) { pr[3] = ta[3] + tb[3]; pr[2] = ta[2] + tb[2]; OPAQUE(pr[3]); OPAQUE(pr[2]); }
      if constexpr (k == 2) { pr[2] += dpp<0x13C>(pr[3]); pr[1] = ta[1] + tb[1]; OPAQUE(pr[2]); OPAQUE(pr[1]); }
      if constexpr (k == 4) { pr[1] += dpp<0x13C>(pr[2]); OPAQUE(pr[1]); }
    } else if constexpr (k == 6) {
      const unsigned o = pk_bf16(ta[0], tb[0]);
      *(LAS u16*)(size_t)(za + c * 512) = (u16)o;
      *(LAS u16*)(size_t)(za + c * 512 + 512) = (u16)(o >> 16);
    } else if constexpr (k & 1) {
      ta[q] += dpp<0x13C>(u0); tb[q] += dpp<0x13C>(u1); OPAQUE(ta[q]); OPAQUE(tb[q]);
    } else {
      u0 = last ? pr[q] : ta[q]; u1 = last ? ta[q] : tb[q]; pr[q] = tb[q]; OPAQUE(u0); OPAQUE(u1);
    }
  };
  SCHED0();
  static_for<(HI - LO + 1) / 2>([&](auto C_) {
    constexpr int c = LO + 2 * C_.value, nm = c + 2, g0 = nm > 10 ? 4 : nm > 7 ? nm - 7 : 0;
    hook(IC<c>{});
    static_for<nm>([&](auto J_) { constexpr int j = J_.value, n = c / 2 - j / 2;
      z[j & 1] = mfma16(G[c + (j & 1) - 2 * n], Fh[n], j < 2 ? zero : z[j & 1]);
      if constexpr (j < 2 && c + 2 <= HI) G_RD(c + 2 + j);
      if constexpr (c > 0) static_for<7>([&](auto K_) { if constexpr (j == (g0 + K_.value < nm ? g0 + K_.value : nm - 1)) fin(IC<c - 2>{}, K_); });
      if constexpr (j == nm / 2) hook(IC<c + 1>{});
      SCHED0(); });
    keep();
  });
  static_for<7>([&](auto K_) { fin(IC<HI - 1>{}, K_); });
}

__global__ __launch_bounds__(512, 2) void k_all(const u16* __restrict__ x, const u16* __restrict__ kk, const u16* __restrict__ vv,
                                                const u16* __restrict__ shk, const u16* __restrict__ ssmk, const u16* __restrict__ ssmd,
                                                const u16* __restrict__ w1, const u16* __restrict__ w3, const u16* __restrict__ w2,
                                                u16* __restrict__ out, char* __restrict__ ws) {
  extern __shared__ __attribute__((aligned(16))) char smem[];
  LAS char* sm = (LAS char*)smem;
  int tid = threadIdx.x, lane = tid & 63;
  const int wid = __builtin_amdgcn_readfirstlane(tid >> 6);
  const int bid = blockIdx.x, xcd = bid & 7, rr = bid >> 3;
  const int mb = (xcd & 1) * 4 + (rr & 3), nb = (xcd >> 1) * 8 + (rr >> 2);
  const int wm = wid & 3, wn = wid >> 2;
  int g = lane >> 4, fr = lane & 15, qq = fr >> 2, pp = lane & 3;
  const __amdgpu_buffer_rsrc_t rs = __builtin_amdgcn_make_buffer_rsrc(ws, 0, WS_END, 0x00020000);
  const unsigned lds0 = (unsigned)(size_t)sm;

  const char* gA[4];
#pragma unroll
  for (int i = 0; i < 4; ++i) gA[i] = (const char*)x + (size_t)(mb * 256 + i * 64 + wid * 8) * (D_ * 2);
  const char* gG = (const char*)w1 + ((size_t)(wid * 4) * F_ + nb * 112) * 2;
  const char* gU = (const char*)w3 + ((size_t)(wid * 4) * F_ + nb * 112) * 2;
  const unsigned vA = (lane >> 3) * (D_ * 2) + (((lane & 7) ^ ((wid & 1) * 4 + g)) * 16);
  const int tq = min(wid * 4 + (lane & 3), 27);
  const unsigned vB = lane < 56 ? (lane / 14) * (F_ * 2) + (lane % 14) * 16 : lane < 60 ? (tq / 14) * (F_ * 2) + (tq % 14) * 128 : (wid * 4 + (lane & 3)) * 2048;
  const unsigned wto = (TW * 32 + rr * 2) * (F_ * 2) + (xcd >> 1) * 1792;
  const unsigned lA = lds0 + wid * 1024, lB = lds0 + wid * U_GS;
#define A_DMA(q, i) dma16<((q) >> 1) * 128, abuf((q) >> 1) + (((q) & 1) * 2 + (i)) * 8192>(gA[((q) & 1) * 2 + (i)], vA, lA)
#define W_DMA(slot, i, NL) do { \
    if (i == 0) { dma16_m<(slot) * U_WS, NL>(gG, vB, lB); gG += 32 * F_ * 2; } \
    else { dma16_m<(slot) * U_WS + U_UP, NL>(gU, vB, lB); gU += 32 * F_ * 2; } } while (0)
#define G1_DMA() do { A_DMA(0, 0); A_DMA(0, 1); A_DMA(1, 0); A_DMA(1, 1); W_DMA(0, 0, 56); W_DMA(0, 1, 56); W_DMA(1, 0, 56); W_DMA(1, 1, 56); } while (0)
#define G2_DMA() do { A_DMA(2, 0); A_DMA(2, 1); A_DMA(3, 0); A_DMA(3, 1); W_DMA(2, 0, 56); W_DMA(2, 1, 56); } while (0)
  const int tb = bid >> 4, cb = bid & 15;
  f32x4 acc[4][7];
  u32x4 d[2], kr[5], vr[2];
  unsigned uo[2][4];
  const char* xrow = (const char*)x + (bid * 8 + wid) * (D_ * 2);
  G1_DMA();
#pragma unroll
  for (int i = 0; i < 28; ++i) { acc[i & 3][i >> 2] = (f32x4){0.f, 0.f, 0.f, 0.f}; OPAQUE(acc[i & 3][i >> 2]); }
  if (tid == 0) *(LAS u32x4*)(sm + L_CNT) = (u32x4){0u, 0u, 0u, 0u};
  unsigned ra[2][2], rw[2];
  ra[0][0] = lds0 + L_A + (wm * 64 + fr) * 128 + ((g ^ (fr >> 1)) * 16);
  ra[0][1] = ra[0][0] ^ 64;
  ra[1][0] = ra[0][0] + 65536; ra[1][1] = ra[0][1] + 65536;
  rw[0] = lds0 + 2 * g * U_GS + qq * 224 + wn * 112 + pp * 8;
  rw[1] = lds0 + 2 * g * U_GS + qq * 224 + wn * 112 + 96 + (pp & 1) * 8 + (pp >> 1) * U_UP;
  OPAQUE(ra[0][0]); OPAQUE(ra[0][1]); OPAQUE(ra[1][0]); OPAQUE(ra[1][1]); OPAQUE(rw[0]); OPAQUE(rw[1]);
  SCHED0();
  G2_DMA();
#define U_RD(i, X, Wl, Wh, t) do { \
    if constexpr (i >= 2 && i < 6) X[i - 2] = *(LAS s16x8*)(size_t)(ra[((t) >> 1) % 3 == 1][(t) & 1] + (((t) >> 1) % 3 == 2 ? 32768 : 0) + (i - 2) * 2048); \
    else { constexpr int j_ = i < 2 ? i : i - 4, c_ = j_ >> 1; \
      LAS char* p_ = (LAS char*)(size_t)(rw[c_ == 6] + ((t) & 3) * U_WS + (c_ == 6 ? 0 : (c_ >> 1) * 32 + (c_ & 1) * U_UP) + (j_ & 1) * U_GS); \
      if constexpr (j_ & 1) Wh[c_] = tr_read(p_); else Wl[c_] = tr_read(p_); } } while (0)
  float cs[7] = {0.f, 0.f, 0.f, 0.f, 0.f, 0.f, 0.f};
  s16x8 X[2][4];
  s16x4 Wl[2][7], Wh[2][7];
  WAIT_V(8); BAR();
  static_for<18>([&](auto I_) { constexpr int i = I_.value; U_RD(i, X[0], Wl[0], Wh[0], 0); });
  unsigned ca = lds0 + wn * U_UP + mb * U_GS + g * 224 + wm * 56 + fr * 4;
  OPAQUE(ca);
  unsigned cw = *(LAS unsigned*)(size_t)ca;
  float c0 = 0.f, c1 = 0.f;
  SCHED0(); WAIT_VL(6); BAR(); SCHED0();

  const int cch = wid & 3;
  unsigned tag = 0, fu = 0, dsk = 0, ep;
  u32x2 skr;
  u32x4 sg, cg[2];
#define U_DMA(s, i) do { \
    if constexpr ((s) < 28 && (i) < 2) W_DMA((s) & 3, (i), 56); \
    if constexpr ((s) < 27 && (i) >= 2) A_DMA((s) + 5, (i) - 2); \
    if constexpr ((s) == 27 && (i) >= 2) { OPAQUE(tid); const unsigned l_ = tid & 63; \
      dma16_s<((i) - 2) * 1024, ((i) - 2) * 1024 + L_T + 32>((const char*)ssmk + (size_t)(bid * 4 + cch) * 4096 + wn * 2048, l_ * 16, lds0 + cch * C_TS + wn * 2048); } } while (0)
  float ax, ay, bx, by, px, py, qx, qy, mean;
  f32x4 tx, ty;
#define PIN(a, b) do { OPAQUE(a); OPAQUE(b); } while (0)
#define UP2(a, b, v) do { a = bflo(v); b = bfhi(v); PIN(a, b); } while (0)
  auto side = [&](auto S_c, auto I_) { constexpr int s = S_c.value, i = I_.value;
    constexpr bool slot = (i & 3) == 0 || (i & 3) == 3;
    if constexpr (s == 4) {
      if constexpr (i == 0) { LANDED(ep); LANDED(skr); tag = __builtin_amdgcn_readfirstlane(ep) + 0x9E3779B9u; }
      if constexpr (i == 3) UP2(tx[0], tx[1], skr[0]);
      if constexpr (i == 4) UP2(tx[2], tx[3], skr[1]);
      if constexpr (i == 8) { OPAQUE(tid); *(LAS f32x4*)(sm + L_SK + (tid & 63) * 16) = tx; }
    }
    if constexpr (slot && s >= S_ST && s < S_ST + 4) {
      constexpr int n = (s - S_ST) * 14 + (i >> 1), r = n < 22 ? n - 16 : n - 46;
      if constexpr (n == 0) { LANDED(d[0]); LANDED(d[1]); }
      if constexpr (n == 22) PIN(d[0], d[1]);
      if constexpr (n < 16) {
        if constexpr (!(n & 1)) UP2(ax, ay, d[n >> 3][(n >> 1) & 3]);
        else { if constexpr (n == 1) { px = ax; py = ay; } else { px += ax; py += ay; } PIN(px, py); }
      }
      if constexpr (n >= 22 && n < 46) {
        constexpr int j = (n - 22) / 3, k = (n - 22) % 3;
        if constexpr (k == 0) UP2(ax, ay, d[j >> 2][j & 3]);
        if constexpr (k == 1) { ax -= mean; ay -= mean; PIN(ax, ay); }
        if constexpr (k == 2) { px = j ? fmaf(ax, ax, px) : ax * ax; py = j ? fmaf(ay, ay, py) : ay * ay; PIN(px, py); }
      }
      if constexpr (r == 0) { px += py; px += dpp<0xB1>(px); OPAQUE(px); }
      if constexpr (r == 1) { px += dpp<0x4E>(px); OPAQUE(px); }
      if constexpr (r == 2) { px += dpp<0x141>(px); OPAQUE(px); }
      if constexpr (r == 3) { px += dpp<0x140>(px); OPAQUE(px); }
      if constexpr (r == 4) { px = add16(px, px); OPAQUE(px); }
      if constexpr (r == 5) { px = add32(px, px) * (1.0f / D_); OPAQUE(px); if constexpr (n < 22) mean = px; }
      if constexpr (r == 6) { px = rbf(px) + 1e-5f; OPAQUE(px); }
      if constexpr (r == 7) { px = __builtin_amdgcn_sqrtf(rbf(px)); OPAQUE(px); }
      if constexpr (r == 8) { px = rbf(px); mean = rbf(mean); PIN(px, mean); }
      if constexpr (r == 9) {
        OPAQUE(tid);
        ST_SC1(((u32x4){__float_as_uint(mean), tag, __float_as_uint(px), tag}), WS_ST + (bid * 8 + wid) * 16 + (tid & 63) * 0x2000000);
      }
    }
    if constexpr (i == 27 && s >= S_KV - 1 && s < S_KV + 3) {
      OPAQUE(tid);
      tx = *(LAS f32x4*)(sm + L_SK + (tid & 7) * 128 + (s + 1 - S_KV) * 32);
      ty = *(LAS f32x4*)(sm + L_SK + (tid & 7) * 128 + (s + 1 - S_KV) * 32 + 16);
    }
    if constexpr (slot && s >= S_KV && s < S_KV + 4) {
      constexpr int e = s - S_KV, k = i >> 1;
      if constexpr (e == 0 && k == 0) { LANDED(kr[0]); LANDED(kr[1]); LANDED(kr[2]); LANDED(kr[3]); LANDED(kr[4]); }
      if constexpr (k == 0) UP2(ax, ay, kr[4][e]);
      if constexpr (k == 1) UP2(bx, by, kr[3][e]);
      if constexpr (k == 2) { qx = ax * tx[0]; qy = ay * ty[0]; PIN(qx, qy); }
      if constexpr (k == 3) { px = bx * tx[0]; py = by * ty[0]; PIN(px, py); }
      if constexpr (k == 4) UP2(ax, ay, kr[2][e]);
      if constexpr (k == 5) { qx = fmaf(bx, tx[1], qx); qy = fmaf(by, ty[1], qy); PIN(qx, qy); }
      if constexpr (k == 6) { px = fmaf(ax, tx[1], px); py = fmaf(ay, ty[1], py); PIN(px, py); }
      if constexpr (k == 7) UP2(bx, by, kr[1][e]);
      if constexpr (k == 8) { qx = fmaf(ax, tx[2], qx); qy = fmaf(ay, ty[2], qy); PIN(qx, qy); }
      if constexpr (k == 9) { px = fmaf(bx, tx[2], px); py = fmaf(by, ty[2], py); PIN(px, py); }
      if constexpr (k == 10) UP2(ax, ay, kr[0][e]);
      if constexpr (k == 11) { qx = fmaf(bx, tx[3], qx); qy = fmaf(by, ty[3], qy); PIN(qx, qy); }
      if constexpr (k == 12) { px = fmaf(ax, tx[3], px); py = fmaf(ay, ty[3], py); PIN(px, py); }
      if constexpr (k == 13) { uo[0][e] = pk_bf16(px, py); uo[1][e] = pk_bf16(qx, qy); PIN(uo[0][e], uo[1][e]); }
    }
    if constexpr (slot && s >= S_V + 5 && s < S_V + 7) {
      constexpr int e = (s - S_V - 5) * 2 + (i >> 1) / 7, k = (i >> 1) % 7;
      if constexpr (e == 0 && k == 0) { LANDED(vr[0]); LANDED(vr[1]); }
      if constexpr (k == 0) UP2(ax, ay, uo[0][e]);
      if constexpr (k == 1) UP2(px, py, vr[0][e]);
      if constexpr (k == 2) UP2(bx, by, uo[1][e]);
      if constexpr (k == 3) UP2(qx, qy, vr[1][e]);
      if constexpr (k == 4) { px *= ax; py *= ay; PIN(px, py); }
      if constexpr (k == 5) { qx *= bx; qy *= by; PIN(qx, qy); }
      if constexpr (k == 6) { uo[0][e] = pk_bf16(px, py); uo[1][e] = pk_bf16(qx, qy); PIN(uo[0][e], uo[1][e]); }
      if constexpr (i == 27) {
        OPAQUE(tid);
        __builtin_amdgcn_raw_buffer_store_b128((u32x4){uo[0][e - 1], uo[0][e], uo[1][e - 1], uo[1][e]}, rs, (tid & 7) * (2 * S_ * 8) + (tid >> 3) * 16,
                                               WS_U + ((cb * 16 + (e >> 1)) * S_ + tb * 128) * 8, 16);
      }
    }
    if constexpr (i == 20) {
      if constexpr (wt(s)) touch_hi<56>((const char*)(s & 1 ? w3 : w1) + wto + (s & ~1) * 32 * (F_ * 2), vB, lds0 + L_JUNK);
      if constexpr (s == S_CL || s == S_CL + 1) { OPAQUE(tid); __builtin_amdgcn_raw_buffer_store_b128((u32x4){~0u, ~0u, ~0u, ~0u}, rs, tid * 16, WS_EX + bid * 16384 + (s - S_CL) * 8192, 16); }
      if constexpr (s == 0) {
        const char* pc;
        asm volatile("s_getpc_b64 %0" : "=s"(pc));
        touch<2>(pc + min(rr * 8 + wid, CODE_BYTES / 256 - 1) * 256, vB, lds0 + L_JUNK);
        OPAQUE(tid);
        asm volatile("global_load_dwordx2 %0, %1, %2 ; HID %0" : "=&v"(skr) : "v"((tid & 63) * 8), "s"((const char*)shk + cb * 512));
        asm volatile("v_mov_b32 %0, 0\n\tglobal_load_dword %0, %0, %1 sc1 ; HID %0" : "=&v"(ep) : "s"(ws + WS_EP));
      }
      if constexpr (s == S_X0 || s == S_X1) { OPAQUE(tid); LD_HID(d[s - S_X0], (tid & 63) * 16, xrow, (s - S_X0) * 1024, ""); }
      if constexpr (s == S_KA || s == S_KB) {
        OPAQUE(tid);
        const __amdgpu_buffer_rsrc_t rk = __builtin_amdgcn_make_buffer_rsrc((void*)kk, 0, S_ * D_ * 2, 0x00020000);
        const unsigned o = (tid >> 3) * (D_ * 4) + (tid & 7) * 16 + (tb * 128 - 3) * (D_ * 2) + cb * 128;
        if constexpr (s == S_KA) { LD_HIDB(kr[4], o + 4 * D_ * 2, rk, " nt"); LD_HIDB(kr[2], o + 2 * D_ * 2, rk, " nt"); LD_HIDB(kr[0], o, rk, " nt"); }
        else { LD_HIDB(kr[3], o + 3 * D_ * 2, rk, " nt"); LD_HIDB(kr[1], o + D_ * 2, rk, " nt"); }
      }
      if constexpr (s == S_V || s == S_V + 1) {
        OPAQUE(tid);
        LD_HID(vr[s - S_V], (tid >> 3) * (D_ * 4) + (tid & 7) * 16, (const char*)vv + tb * 128 * (D_ * 2) + cb * 128, (s - S_V) * (D_ * 2), " nt");
      }
      if constexpr (s == S_FL) { OPAQUE(tid); __builtin_amdgcn_raw_buffer_store_b32(tag, rs, WS_FU + (cb * 16 + tb) * 128 + min(tid, 1) * 0x2000000, 0, 16); }
    }
    if constexpr (i == 24) {
      if constexpr (w2t(s)) touch_hi<60>((const char*)w2 + ((s - W2T) / 2 * 16 + (bid >> 4)) * (32 * D_ * 2) + (bid & 15) * 128, vB, lds0 + L_JUNK);
    }
  };
  static_for<31>([&](auto S_c) {
    constexpr int s = S_c.value, b = s & 1;
    if constexpr (s == 28) {
      OPAQUE(tid);
      sg = LD_SC1(WS_ST + (mb * 256 + (tid & 255)) * 16);
      fu = __hip_atomic_load((GAS unsigned*)(ws + WS_FU + ((bid >> 4) * 16 + (tid & 15)) * 128), RLX);
      dsk = __hip_atomic_load((GAS u16*)(ssmd + bid * 4 + (tid & 3)), RLX);
    }
    if constexpr (s == 30) {
      OPAQUE(tid);
#pragma unroll
      for (int k = 0; k < 2; ++k) cg[k] = LD_SC1(WS_CS + nb * 16384 + k * 8192 + tid * 16);
    }
    SCHED0();
    static_for<28>([&](auto I_) { constexpr int i = I_.value, c = i >> 2, m = i & 3;
      mfma_acc(acc[m][c], cat8(Wl[b][c], Wh[b][c]), X[b][m]);
      if constexpr (i < 18) U_RD(i, X[b ^ 1], Wl[b ^ 1], Wh[b ^ 1], s + 1);
      if constexpr ((i & 3) == 3 && i >= DM0 && i < DM0 + 16) U_DMA(s, (i - DM0) >> 2);
      if constexpr (s == 0 && i < 6 && i % 3 != 2) { if constexpr (i < 2) W_DMA(3, i, 56); else A_DMA(4, i - 3); }
      side(S_c, I_);
      if constexpr (s < CS0) {
        if constexpr (i == 1) { c0 += bflo(cw); OPAQUE(c0); }
        if constexpr (i == 2) { c1 += bfhi(cw); OPAQUE(c1); }
        if constexpr (i == 18 && s < CS0 - 1) cw = *(LAS unsigned*)(size_t)(ca + ((s + 1) & 3) * U_WS);
        if constexpr (s == CS0 - 1) {
          if constexpr (i == 5) { c0 = add32(c0, c1); OPAQUE(c0); }
          if constexpr (i == 9) { c0 = add16(c0, c0); OPAQUE(c0); }
          if constexpr (i == 21) {
            OPAQUE(tid);
            __builtin_amdgcn_raw_buffer_store_b64((u32x2){__float_as_uint(c0), tag}, rs, (tid & 15) * 16 + (tid & 32) / 4 + (((tid & 15) + 2) >> 4) * 0x2000000,
                                                  WS_CS + (nb * 8 + mb) * 2048 + wn * 1024 + wm * 224, 16);
          }
        }
      }
      if constexpr ((m == 1 || m == 2) && s >= CS0) colsum<m - 1>(cs[c], cat8(Wl[b][c], Wh[b][c]), wm);
      if constexpr (s == 30 && i >= 14) colsum<i & 1>(cs[(i - 14) >> 1], cat8(Wl[1][(i - 14) >> 1], Wh[1][(i - 14) >> 1]), wm);
      SCHED0(); });
    if constexpr (s < 30) {
      WAIT_VL((s == 1 ? 10 : s == 28 ? 7 : s == 29 ? 5 : 8) + extra(s - 2) + extra(s - 1) + extra(s));
      BAR(); SCHED0();
    }
  });
  OPAQUE(tid); lane = tid & 63; g = lane >> 4; fr = lane & 15; qq = fr >> 2; pp = lane & 3;
  const int mb2 = mb * 2 + ((rr >> 2) & 1);
  const int kh = xcd >> 2, nb2 = ((xcd >> 1) & 1) * 4 + (rr >> 3), pbid = bid ^ 4;

  const char* wB = (const char*)w2 + ((size_t)(cch * 8) * D_ + nb2 * 128) * 2;
  const char* gB = wB + (size_t)(kh * NT + wn * PRE) * (32 * D_ * 2);
  const unsigned v2 = g * (D_ * 2) + ((fr ^ ((g | ((wid & 1) << 2)) << 1)) * 16);
  const unsigned dB = lds0 + cch * 2048;
#define D_STAGE(d, o) do { dma16<0, (o)>(gB, v2, d); dma16<0, (o) + 1024>(gB + 4 * D_ * 2, v2, d); gB += 32 * D_ * 2; } while (0)
  u32x4 ur[2];
  f32x4 cv[7];
  f32x2 st[4];
  {
    float r[8], r2[8];
    SCHED0();
    static_for<28>([&](auto I_) { constexpr int i = I_.value, c = i >> 2, m = i & 3;
      mfma_acc(acc[m][c], cat8(Wl[1][c], Wh[1][c]), X[1][m]);
      if constexpr (i == 1) {
        *(LAS float*)(sm + L_CSP + wid * 512 + lane * 4) = add16(add32(cs[0], cs[1]), add32(cs[2], cs[3]));
        *(LAS float*)(sm + L_CSP + wid * 512 + 256 + lane * 4) = add16(add32(cs[4], cs[5]), add32(cs[6], 0.f));
      }
      if constexpr (i == 4) {
        for (unsigned spins = 0; !__all(sg[1] == tag && sg[3] == tag) && spins < SPIN_CAP; ++spins) {
          __builtin_amdgcn_s_sleep(2); FENCE_C();
          sg = LD_SC1(WS_ST + (mb * 256 + (tid & 255)) * 16);
        }
        *(LAS u32x2*)(sm + L_ST + (tid & 255) * 8) = (u32x2){sg[0], sg[2]};
        wait_flags((GAS unsigned*)(ws + WS_FU + ((bid >> 4) * 16 + fr) * 128), fu, tag);
#pragma unroll
        for (int k = 0; k < 2; ++k) ur[k] = __builtin_amdgcn_raw_buffer_load_b128(rs, WS_U + bid * 16384 + tid * 32 + k * 16, 0, 16);
      }
      if constexpr (i == 3) {
        for (unsigned spins = 0; !__all(lane >= 56 || (cg[0][1] == tag && cg[0][3] == tag && cg[1][1] == tag && cg[1][3] == tag)) && spins < SPIN_CAP; ++spins) {
          __builtin_amdgcn_s_sleep(2); FENCE_C();
#pragma unroll
          for (int k = 0; k < 2; ++k) cg[k] = LD_SC1(WS_CS + nb * 16384 + k * 8192 + tid * 16);
        }
        *(LAS f32x2*)(sm + L_CSG + tid * 8) = (f32x2){__uint_as_float(cg[0][0]) + __uint_as_float(cg[1][0]), __uint_as_float(cg[0][2]) + __uint_as_float(cg[1][2])};
      }
      if constexpr (i == 6) {
        if (wid < 4 && lane == wid) {
          LAS char* p = sm + L_T + wid * C_TS;
          *(LAS u32x4*)p = (u32x4){0u, 0u, 0u, 0u}; *(LAS u32x4*)(p + 16) = (u32x4){0u, 0u, 0u, 0u};
          *(LAS u16*)(p + 32) = (u16)pk_bf16(bflo(*(LAS u16*)(p + 32)) + bflo(dsk), 0.f);
        }
      }
      if constexpr (i == 10) {
        WAIT_L0(); BAR();
#pragma unroll
        for (int k = 0; k < 8; ++k) r[k] = *(LAS float*)(sm + L_CSP + wn * 2048 + k * 256 + lane * 4);
#pragma unroll
        for (int k = 0; k < 4; ++k) st[k] = *(LAS f32x2*)(sm + L_ST + (wm * 64 + k * 16 + fr) * 8);
        LAS char* q = sm + L_CSG + wn * 224;
#pragma unroll
        for (int k = 0; k < 4; ++k) {
          r2[2 * k] = *(LAS float*)(q + k * 1024 + (g >> 1) * 512 + (lane & 31) * 4);
          r2[2 * k + 1] = *(LAS float*)(q + k * 1024 + 128 + (g & 1 ? (fr >> 3) * 512 + 64 + (fr & 7) * 4 : (g >> 1) * 512 + fr * 4));
        }
      }
      if constexpr (i == 16) {
        LAS char* q = sm + L_CSF + wid * 512;
        *(LAS float*)(q + (((g & 1) << 1) | (g >> 1)) * 64 + fr * 4) = ((r[0] + r[2]) + (r[4] + r[6])) + ((r2[0] + r2[2]) + (r2[4] + r2[6]));
        *(LAS float*)(q + (((g & 1) << 1) | (g >> 1)) * 64 + 256 + fr * 4) = ((r[1] + r[3]) + (r[5] + r[7])) + ((r2[1] + r2[3]) + (r2[5] + r2[7]));
#pragma unroll
        for (int c2 = 0; c2 < 7; ++c2) cv[c2] = *(LAS f32x4*)(q + c2 * 64 + g * 16);
      }
      if constexpr (i == 23) D_STAGE(dB + wn * (PRE * D_SLOT), 0);
      if constexpr (i == 20) {
        if (tid < 64) *(LAS u32x4*)(sm + L_U + g * C_US + fr * 16) = (u32x4){0u, 0u, 0u, 0u};
      }
      SCHED0(); });
  }
  MFMA_DONE();

  {
    float ea[4], s2[4];
#pragma unroll
    for (int m = 0; m < 4; ++m) { ea[m] = -1.44269504089f * __builtin_amdgcn_rcpf(st[m][1]); s2[m] = st[m][1] * st[m][1]; }
    auto sw = [](f32x2 a, f32x2 b, float e, float q) {
      const f32x2 t = a * e;
      const f32x2 d = (f32x2){__builtin_amdgcn_exp2f(t[0]), __builtin_amdgcn_exp2f(t[1])} * q + q;
      const f32x2 h = a * b * (f32x2){__builtin_amdgcn_rcpf(d[0]), __builtin_amdgcn_rcpf(d[1])};
      return pk_bf16(h[0], h[1]);
    };
    const unsigned hb = WS_H + ((nb * 14 + wn * 7) * S_ + mb * 256 + wm * 64) * 16;
    const unsigned ho = ((g >> 1) * S_ + (g & 1) * 16 + fr) * 16;
#pragma unroll
    for (int P = 0; P < 3; ++P)
#pragma unroll
      for (int mp = 0; mp < 2; ++mp) {
        u32x2 h[2];
#pragma unroll
        for (int i = 0; i < 2; ++i) {
          const int m = 2 * mp + i;
          const f32x4 ga = acc[m][2 * P] - st[m][0] * cv[2 * P], up = acc[m][2 * P + 1] - st[m][0] * cv[2 * P + 1];
          h[i] = (u32x2){sw(ga.xy, up.xy, ea[m], s2[m]), sw(ga.zw, up.zw, ea[m], s2[m])};
        }
        ST_SC1(swap16(h[0], h[1]), hb + ho + (2 * P * S_ + mp * 32) * 16);
        SCHED0();
      }
    u32x2 t6[2];
#pragma unroll
    for (int mp = 0; mp < 2; ++mp) {
      const float e = lane < 32 ? ea[2 * mp] : ea[2 * mp + 1], q = lane < 32 ? s2[2 * mp] : s2[2 * mp + 1];
      f32x4 ga = acc[2 * mp][6] - st[2 * mp][0] * cv[6], up = acc[2 * mp + 1][6] - st[2 * mp + 1][0] * cv[6];
#pragma unroll
      for (int j = 0; j < 4; ++j) {
        auto r = __builtin_amdgcn_permlane32_swap(__float_as_uint(ga[j]), __float_as_uint(up[j]), false, false);
        ga[j] = __uint_as_float(r[0]); up[j] = __uint_as_float(r[1]);
      }
      t6[mp] = (u32x2){sw(ga.xy, up.xy, e, q), sw(ga.zw, up.zw, e, q)};
    }
    ST_SC1(swap16(t6[0], t6[1]), hb + (6 * S_ + ((g & 1) * 2 + (g >> 1)) * 16 + fr) * 16);
  }
#pragma unroll
  for (int c = 0; c < 4; ++c) {
    const unsigned sel = (c & 1) ? 0x07060302u : 0x05040100u;
    *(LAS u32x2*)(sm + L_U + c * C_US + 256 + tid * 8) =
        (u32x2){__builtin_amdgcn_perm(ur[0][2 + (c >> 1)], ur[0][c >> 1], sel), __builtin_amdgcn_perm(ur[1][2 + (c >> 1)], ur[1][c >> 1], sel)};
  }

  LAS char* tp = sm + L_T + cch * C_TS + 32 + (-12 + 4 * pp + 4 * (31 - 8 * g - qq)) * 2;
  LAS char* gp = sm + L_U + cch * C_US + 256 + (16 * pp - 112 + 4 * (8 * g + qq)) * 2;
  LAS char* zp = sm + L_Z + lane * 8 + cch * 2;
  if (wid < 4) {
    gB = wB + (size_t)(kh * NT + 2 * PRE) * (32 * D_ * 2);
#define D_GROUP(j) do { const unsigned d_ = dB + (((j) + 3) & 3) * (4 * D_SLOT);     \
      D_STAGE(dB + (((j) + 2) & 3) * (4 * D_SLOT), 3 * D_SLOT); \
      if ((j) + 3 == N1 / 4) gB = wB + (size_t)N1 * (32 * D_ * 2); \
      D_STAGE(d_, 0); D_STAGE(d_, D_SLOT); D_STAGE(d_, 2 * D_SLOT); } while (0)
    conv_part<0, CSPLIT - 1>(tp, gp, zp, lane == 63, [&](auto P_) { constexpr int p = P_.value;
      if constexpr (p == 0) __builtin_amdgcn_raw_buffer_store_b32(tag, rs, WS_FH + (mb * 32 + nb) * 128 + min(tid, 1) * 0x2000000, 0, 16);
      if constexpr (p == LND) { WAIT_V(2 * (staged(p) - 8)); if (lane == 0) lds_add(sm + L_CNT); }
      static_for<staged(p + 1) - staged(p)>([&](auto I_) { D_STAGE(dB, (staged(p) + I_.value) * D_SLOT); }); });
    FENCE_C();
    if (lane == 0) lds_add(sm + L_CNT + 8);
    lds_wait(sm + L_CNT + 8, 8);
    D_STAGE(dB, 15 * D_SLOT);
    {
      u32x4 o[4];
#pragma unroll
      for (int i = 0; i < 4; ++i) o[i] = *(LAS u32x4*)(sm + L_Z + i * 4096 + tid * 16);
#pragma unroll
      for (int i = 0; i < 4; ++i) ST_SC1(o[i], WS_Z + bid * 16384 + i * 4096 + tid * 16);
    }
    WAIT_V(6); BAR();
    static_for<7>([&](auto I_) { D_STAGE(dB, I_.value * D_SLOT); });
    WAIT_V(18); BAR();
    D_GROUP(3);
    for (int j = 4; j < NT / 4 - 3; ++j) {
      WAIT_V(14); BAR();
      if (j == 4 && tid == 0) __hip_atomic_store((GAS unsigned*)(ws + WS_FZ + bid * 128), tag, RLX);
      D_GROUP(j);
    }
    WAIT_V(14); BAR();
    D_STAGE(dB, ((NT - 1) & 15) * D_SLOT);
    WAIT_V(8); BAR();
    WAIT_V(0); BAR();
    if (bid == 0 && wid == 3) {
#pragma unroll
      for (int i = 0; i < 4; ++i) { GAS unsigned* fp = (GAS unsigned*)(ws + WS_FU + (i * 64 + lane) * 128); wait_flags(fp, __hip_atomic_load(fp, RLX), tag); }
      if (lane == 0) __hip_atomic_store((GAS unsigned*)(ws + WS_EP), tag, RLX);
    }
    return;
  }

  __builtin_amdgcn_s_setprio(1);
  unsigned fh;
  conv_part<CSPLIT, 31>(tp, gp, zp, lane == 63, [&](auto P_) {
    if constexpr (P_.value == FHP) {
      OPAQUE(tid); const int l = tid & 63;
      fh = __builtin_amdgcn_raw_buffer_load_b32(rs, (l < 19 ? kh * 13 + l : nb ^ 16) * 128, WS_FH + mb * 32 * 128, 16);
    } });
  FENCE_C();
  if (lane == 0) lds_add(sm + L_CNT + 8);
  const int row0 = mb2 * 128 + kh * 64 + cch * 16 + fr;
  u32x4 xv[4];
  {
    GAS unsigned* fp = (GAS unsigned*)(ws + (lane < 19 ? WS_FH + (mb * 32 + kh * 13 + lane) * 128 : WS_FH + (mb * 32 + (nb ^ 16)) * 128));
    wait_flags(fp, fh, tag);
  }
  const unsigned vK = WS_H + (g * S_ + row0) * 16, vS = vK ^ 1024;
  unsigned ko = kh * NT * (4 * S_ * 16);
  u32x4 Hf[8][2];
#define H_LD(t, m) Hf[(t) & 7][m] = __builtin_amdgcn_raw_buffer_load_b128(rs, (m) ? vS : vK, ko, 16)
  static_for<8>([&](auto I_) { H_LD(I_.value, 0); H_LD(I_.value, 1); ko += 4 * S_ * 16; });
  unsigned rb[2][8];
#pragma unroll
  for (int c = 0; c < 8; ++c) {
    rb[0][c] = lds0 + (8 * g + qq) * 256 + ((c ^ (qq | ((g & 1) << 2))) * 32) + pp * 8;
    rb[1][c] = rb[0][c] + 8 * D_SLOT; OPAQUE(rb[0][c]); OPAQUE(rb[1][c]);
  }
  s16x4 Vl[2][8], Vh[2][8];
#define D_RD(i, b, slot) do { LAS char* p_ = (LAS char*)(size_t)(rb[((slot) >> 3) & 1][(i) >> 1] + ((slot) & 7) * D_SLOT + ((i) & 1) * 1024); \
    if constexpr ((i) & 1) Vh[b][(i) >> 1] = tr_read(p_); else Vl[b][(i) >> 1] = tr_read(p_); } while (0)
#define D_STEP(s, NM, LD, RD, FI) do { constexpr int b_ = (s) & 1; \
    if constexpr ((s) == 3 && (FI)) { asm volatile("s_cmp_eq_u32 %0, 0\n\ts_cbranch_scc1 1f\n\ts_barrier\n1:" ::"s"(it) : "scc", "memory"); } \
    else if constexpr (((s) & 3) == 3 && RD) BAR(); \
    SCHED0(); \
    static_for<8 * NM>([&](auto I_) { constexpr int i = I_.value, c = NM == 2 ? i >> 1 : i, m = NM == 2 ? i & 1 : 0; \
      if constexpr (c % WGR == 0 && m == 0 && RD) __builtin_amdgcn_s_waitcnt(0xC07F | (16 - 2 * WGR) << 8); \
      mfma_acc(ac[m][c], cat8(Vl[b_][c], Vh[b_][c]), __builtin_bit_cast(s16x8, Hf[(s) & 7][m])); \
      if constexpr (RD) { if constexpr (NM == 2) D_RD(i, b_ ^ 1, (s) + 1); else { D_RD(2 * i, b_ ^ 1, (s) + 1); D_RD(2 * i + 1, b_ ^ 1, (s) + 1); } } \
      if constexpr (c == 7 && m < LD) H_LD(s, m); \
      SCHED0(); }); \
    ko += 4 * S_ * 16; } while (0)
  f32x4 ac[2][8];
#pragma unroll
  for (int i = 0; i < 16; ++i) { ac[i >> 3][i & 7] = (f32x4){0.f, 0.f, 0.f, 0.f}; OPAQUE(ac[i >> 3][i & 7]); }
  GAS unsigned* fzp = (GAS unsigned*)(ws + WS_FZ + (nb2 * 32 + (lane & 31)) * 128);
  u32x2 zv[8];
  lds_wait(sm + L_CNT, 4);
  static_for<16>([&](auto I_) { D_RD(I_.value, 0, 0); });
  int it;
  for (it = 0; it < N1 / 16 - 1; ++it) static_for<16>([&](auto J_) { D_STEP(J_.value, 2, 2, true, true); });
  unsigned fz = __hip_atomic_load(fzp, RLX);
  static_for<16>([&](auto J_) { constexpr int s = J_.value;
    if constexpr (s == 8) {
      ko = N1 * (4 * S_ * 16);
      wait_flags(fzp, fz, tag);
#pragma unroll
      for (int c = 0; c < 8; ++c) zv[c] = __builtin_amdgcn_raw_buffer_load_b64(rs, WS_Z + ((nb2 * 32 + c * 4 + g) * S_ + row0) * 8, 0, 16);
    }
    D_STEP(s, 2, (s < 8 ? 2 : 1), true, false); });
  MFMA_DONE();
#pragma unroll
  for (int P = 0; P < 4; ++P) {
    u32x4 o;
#pragma unroll
    for (int i = 0; i < 4; ++i) {
      const f32x4 a = ac[1][2 * P + (i >> 1)];
      o[i] = min(pk_bf16(a[(i & 1) * 2], a[(i & 1) * 2 + 1]), 0xfffffffeu);
    }
    ST_SC1(o, WS_EX + pbid * 16384 + P * 4096 + (tid & 255) * 16);
  }
  u32x4 pv[4];
  static_for<N2>([&](auto J_) { constexpr int s = J_.value;
    if constexpr (s == N2 - 8) {
#pragma unroll
      for (int P = 0; P < 4; ++P) xv[P] = *(const u32x4*)(x + (size_t)row0 * D_ + nb2 * 128 + (2 * P + (g & 1)) * 16 + (g >> 1) * 8);
    }
    if constexpr (s == N2 - 3) {
#pragma unroll
      for (int i = 0; i < 4; ++i) pv[i] = LD_SC1(WS_EX + bid * 16384 + i * 4096 + (tid & 255) * 16);
    }
    D_STEP(s, 1, (s + 8 < N2 ? 1 : 0), (s + 1 < N2), false);
  });
  MFMA_DONE();
  for (unsigned spins = 0; spins < SPIN_CAP; ++spins) {
    unsigned mx = 0;
#pragma unroll
    for (int i = 0; i < 16; ++i) mx = max(mx, pv[i >> 2][i & 3]);
    if (!__any(mx == ~0u)) break;
    __builtin_amdgcn_s_sleep(1); FENCE_C();
#pragma unroll
    for (int i = 0; i < 4; ++i) pv[i] = LD_SC1(WS_EX + bid * 16384 + i * 4096 + (tid & 255) * 16);
  }
  {
    const f32x2 sv = *(LAS f32x2*)(sm + L_ST + (row0 & 255) * 8);
    const float ri = __builtin_amdgcn_rcpf(sv[1]), mo = -sv[0] * ri;
    const __amdgpu_buffer_rsrc_t ro = __builtin_amdgcn_make_buffer_rsrc(out, 0, S_ * D_ * 2, 0x00020000);
#pragma unroll
    for (int P = 0; P < 4; ++P) {
      const u32x4 xs = swap16((u32x2){xv[P][0], xv[P][1]}, (u32x2){xv[P][2], xv[P][3]});
      u32x2 o[2];
#pragma unroll
      for (int i = 0; i < 4; ++i) {
        const int c = 2 * P + (i >> 1), d = i & 1;
        const float x0 = bflo(xs[i]), x1 = bfhi(xs[i]);
        const float y0 = fmaf(fmaf(x0, ri, mo), bflo(zv[c][d]), x0) + (ac[0][c][2 * d] + bflo(pv[P][i]));
        const float y1 = fmaf(fmaf(x1, ri, mo), bfhi(zv[c][d]), x1) + (ac[0][c][2 * d + 1] + bfhi(pv[P][i]));
        o[i >> 1][d] = pk_bf16(y0, y1);
      }
      __builtin_amdgcn_raw_buffer_store_b128(swap16(o[0], o[1]), ro, (row0 * D_ + nb2 * 128 + (2 * P + (g & 1)) * 16 + (g >> 1) * 8) * 2, 0, 16);
    }
  }
  WAIT_V0C();
}

extern "C" void kernel_launch(void* const* d_in, const int* in_sizes, int n_in, void* d_out, int out_size, void* d_ws, size_t ws_size,
                              hipStream_t stream) {
  (void)hipFuncSetAttribute((const void*)k_all, hipFuncAttributeMaxDynamicSharedMemorySize, L_ALLOC);
  k_all<<<256, 512, L_ALLOC, stream>>>((const u16*)d_in[0], (const u16*)d_in[1], (const u16*)d_in[2], (const u16*)d_in[3], (const u16*)d_in[4],
                                     (const u16*)d_in[5], (const u16*)d_in[7], (const u16*)d_in[8], (const u16*)d_in[9], (u16*)d_out, (char*)d_ws);
}
```

```cpp
#include <hip/hip_runtime.h>
#include <hip/hip_bf16.h>

#define LAS __attribute__((address_space(3)))
#define GAS __attribute__((address_space(1)))
typedef unsigned short u16;
typedef short s16x4 __attribute__((ext_vector_type(4)));
typedef short s16x8 __attribute__((ext_vector_type(8)));
typedef __bf16 bf16x2 __attribute__((ext_vector_type(2)));
typedef __bf16 bf16x8 __attribute__((ext_vector_type(8)));
typedef float f32x2 __attribute__((ext_vector_type(2)));
typedef float f32x4 __attribute__((ext_vector_type(4)));
typedef unsigned u32x4 __attribute__((ext_vector_type(4)));
typedef unsigned u32x2 __attribute__((ext_vector_type(2)));

constexpr int S_ = 2048, D_ = 1024, F_ = 3584;

constexpr unsigned WS_EP = 0;
constexpr unsigned WS_FU = 4096;
constexpr unsigned WS_FH = WS_FU + 32768;
constexpr unsigned WS_FZ = WS_FH + 32768;
constexpr unsigned WS_ST = WS_FZ + 32768;
constexpr unsigned WS_CS = WS_ST + 32768;
constexpr unsigned WS_U = 1u << 20;
constexpr unsigned WS_Z = WS_U + S_ * D_ * 2;
constexpr unsigned WS_EX = WS_Z + S_ * D_ * 2;
constexpr unsigned WS_H = WS_EX + 256 * 16384;
constexpr unsigned WS_END = WS_H + S_ * F_ * 2;
static_assert(WS_END <= 0x2000000, "offsets used to drop a store");
constexpr unsigned SPIN_CAP = 1u << 18;

__device__ __forceinline__ float bflo(unsigned v) { return __uint_as_float(v << 16); }
__device__ __forceinline__ float bfhi(unsigned v) { return __uint_as_float(v & 0xffff0000u); }
__device__ __forceinline__ unsigned pk_bf16(float lo, float hi) {
  f32x2 v = {lo, hi};
  return __builtin_bit_cast(unsigned, __builtin_convertvector(v, bf16x2));
}
__device__ __forceinline__ float rbf(float x) { return bflo(pk_bf16(x, x)); }
__device__ __forceinline__ float dot2(unsigned a, unsigned b, float c) {
  return __builtin_amdgcn_fdot2_f32_bf16(__builtin_bit_cast(bf16x2, a), __builtin_bit_cast(bf16x2, b), c, false);
}
template <int CTRL> __device__ __forceinline__ float dpp(float v) {
  return __int_as_float(__builtin_amdgcn_update_dpp(0, __float_as_int(v), CTRL, 0xf, 0xf, true));
}
__device__ __forceinline__ float add32(float a, float b) {
  auto r = __builtin_amdgcn_permlane32_swap(__float_as_uint(a), __float_as_uint(b), false, false);
  return __uint_as_float(r[0]) + __uint_as_float(r[1]);
}
__device__ __forceinline__ float add16(float a, float b) {
  auto r = __builtin_amdgcn_permlane16_swap(__float_as_uint(a), __float_as_uint(b), false, false);
  return __uint_as_float(r[0]) + __uint_as_float(r[1]);
}
__device__ __forceinline__ float sum64(float v) {
  v += dpp<0xB1>(v); v += dpp<0x4E>(v); v += dpp<0x141>(v); v += dpp<0x140>(v);
  v = add16(v, v);
  return add32(v, v);
}
__device__ __forceinline__ u32x4 swap16(u32x2 a, u32x2 b) {
  auto r0 = __builtin_amdgcn_permlane16_swap(a[0], b[0], false, false);
  auto r1 = __builtin_amdgcn_permlane16_swap(a[1], b[1], false, false);
  return (u32x4){r0[0], r1[0], r0[1], r1[1]};
}
__device__ __forceinline__ f32x4 mfma16(s16x8 a, s16x8 b, f32x4 c) {
  return __builtin_amdgcn_mfma_f32_16x16x32_bf16(__builtin_bit_cast(bf16x8, a), __builtin_bit_cast(bf16x8, b), c, 0, 0, 0);
}
__device__ __forceinline__ void mfma_acc(f32x4& c, s16x8 a, s16x8 b) { asm volatile("v_mfma_f32_16x16x32_bf16 %0, %1, %2, %0" : "+v"(c) : "v"(a), "v"(b)); }
#define MFMA_DONE() do { asm volatile("s_nop 7\n\ts_nop 7"); __builtin_amdgcn_sched_barrier(0); } while (0)
__device__ __forceinline__ s16x4 tr_read(LAS char* p) { return __builtin_amdgcn_ds_read_tr16_b64_v4i16((LAS s16x4*)p); }
__device__ __forceinline__ s16x8 cat8(s16x4 a, s16x4 b) { return __builtin_shufflevector(a, b, 0, 1, 2, 3, 4, 5, 6, 7); }
template <int OFF, int DOFF = 0> __device__ __forceinline__ void dma16(const void* base, unsigned voff, unsigned dst) {
  asm volatile("s_add_u32 m0, %0, %4\n\ts_nop 0\n\tglobal_load_lds_dwordx4 %1, %2 offset:%3" ::"s"(dst), "v"(voff), "s"(base), "i"(OFF), "i"(DOFF - OFF) : "scc");
}
template <int OFF, int DOFF> __device__ __forceinline__ void dma16_s(const void* base, unsigned voff, unsigned dst) {
  asm volatile("s_add_u32 m0, %0, %4\n\ts_nop 0\n\tglobal_load_lds_dwordx4 %1, %2 offset:%3" " nt" ::"s"(dst), "v"(voff), "s"(base), "i"(OFF), "i"(DOFF - OFF) : "scc");
}
template <int DOFF, int NL> __device__ __forceinline__ void dma16_m(const void* base, unsigned voff, unsigned dst) {
  asm volatile("s_add_u32 m0, %0, %4\n\ts_bfm_b64 exec, %3, 0\n\tglobal_load_lds_dwordx4 %1, %2\n\ts_mov_b64 exec, -1"
               ::"s"(dst), "v"(voff), "s"(base), "i"(NL), "i"(DOFF) : "scc");
}
template <int NL> __device__ __forceinline__ void touch(const void* base, unsigned v, unsigned dst) {
  unsigned t;
  asm volatile("v_lshlrev_b32 %0, 3, %3\n\ts_mov_b32 m0, %2\n\ts_bfm_b64 exec, %4, 0\n\tglobal_load_lds_dword %0, %1\n\ts_mov_b64 exec, -1"
               : "=&v"(t) : "s"(base), "s"(dst), "v"(v), "i"(NL));
}
template <int L0> __device__ __forceinline__ void touch_hi(const void* base, unsigned v, unsigned dst) {
  asm volatile("s_mov_b32 m0, %1\n\ts_bfm_b64 exec, 4, %3\n\tglobal_load_lds_dword %2, %0\n\ts_mov_b64 exec, -1" :: "s"(base), "s"(dst), "v"(v), "i"(L0));
}
#define LD_HID(d, voff, base, OFF, MOD) asm volatile("global_load_dwordx4 %0, %1, %2 offset:%3" MOD " ; HID %0" : "=&v"(d) : "v"(voff), "s"(base), "i"(OFF))
#define LD_HIDB(d, voff, r, MOD) asm volatile("buffer_load_dwordx4 %0, %1, %2, 0 offen" MOD " ; HID %0" : "=&v"(d) : "v"(voff), "s"(r))
#define LANDED(x) asm volatile("; USE %0" : "+v"(x))
template <int H> __device__ __forceinline__ void colsum(float& cs, s16x8 w, int idx) {
  const u32x4 v = __builtin_bit_cast(u32x4, w);
  unsigned a;
  if constexpr (H == 0)
    asm volatile("s_set_gpr_idx_on %2, gpr_idx(SRC1)\n\tv_lshlrev_b32 %1, 16, %3 ; CSCHK %3 %4\n\ts_set_gpr_idx_off\n\tv_add_f32 %0, %0, %1"
                 : "+v"(cs), "=&v"(a) : "s"(idx), "v"(v[0]), "v"(v));
  else
    asm volatile("s_set_gpr_idx_on %2, gpr_idx(SRC1)\n\tv_and_b32 %1, 0xffff0000, %3 ; CSCHK %3 %4\n\ts_set_gpr_idx_off\n\tv_add_f32 %0, %0, %1"
                 : "+v"(cs), "=&v"(a) : "s"(idx), "v"(v[0]), "v"(v));
}
#define OPAQUE(x) asm volatile("" : "+v"(x))
#define WAIT_V(n) asm volatile("s_waitcnt vmcnt(%0)" ::"i"(n) : "memory")
#define WAIT_L0() __builtin_amdgcn_s_waitcnt(0xC07F)
#define WAIT_V0C() __builtin_amdgcn_s_waitcnt(0x0F70)
#define WAIT_VL(n) do { WAIT_L0(); WAIT_V(n); } while (0)
#define FENCE_C() asm volatile("" ::: "memory")
#define BAR() do { FENCE_C(); __builtin_amdgcn_s_barrier(); FENCE_C(); } while (0)
#define SCHED0() __builtin_amdgcn_sched_barrier(0)
#define RLX __ATOMIC_RELAXED, __HIP_MEMORY_SCOPE_AGENT
#define ST_SC1(v, off) __builtin_amdgcn_raw_buffer_store_b128(v, rs, off, 0, 16)
#define LD_SC1(off) __builtin_amdgcn_raw_buffer_load_b128(rs, off, 0, 16)

template <int V> struct IC { static constexpr int value = V; };
template <int N, int I = 0, class Fn> __device__ __forceinline__ void static_for(Fn&& f) {
  if constexpr (I < N) { f(IC<I>{}); static_for<N, I + 1>(f); }
}

__device__ __forceinline__ void wait_flags(GAS unsigned* p, unsigned v, unsigned tag) {
  if (__builtin_expect(!__all(v == tag), 0)) {
    for (unsigned spins = 0; spins < SPIN_CAP; ++spins) {
      __builtin_amdgcn_s_sleep(2);
      if (__all(__hip_atomic_load(p, RLX) == tag)) break;
    }
  }
  FENCE_C();
}

__device__ __forceinline__ unsigned lds_add(LAS char* p) { return __hip_atomic_fetch_add((LAS unsigned*)p, 1u, __ATOMIC_RELAXED, __HIP_MEMORY_SCOPE_WORKGROUP); }
__device__ __forceinline__ void lds_wait(LAS char* p, unsigned n) {
  FENCE_C();
  for (unsigned spins = 0; __hip_atomic_load((LAS unsigned*)p, __ATOMIC_RELAXED, __HIP_MEMORY_SCOPE_WORKGROUP) != n && spins < SPIN_CAP; ++spins) __builtin_amdgcn_s_sleep(1);
  FENCE_C();
}

constexpr int U_GS = 960;
constexpr int U_UP = 8 * U_GS + 16;
constexpr int U_WS = U_UP + 8 * U_GS;
constexpr int L_A = 61568;
constexpr int L_ST = L_A + 3 * 32768;
constexpr int L_JUNK = L_ST + 2048;
constexpr int L_SK = L_JUNK + 256;
constexpr int L_CNT = L_SK + 1024;
constexpr int L_END = L_CNT + 16;
constexpr int L_ALLOC = L_END;
constexpr int abuf(int u) { return L_A + (u % 3 == 0 ? 0 : u % 3 == 1 ? 65536 : 32768); }
constexpr int CODE_BYTES = 50 * 1024;
constexpr int DM0 = 3, TW = 6, W2T = 11, CS0 = 23;
constexpr int S_X0 = 1, S_X1 = S_X0 + 1, S_ST = S_X1 + 4, S_KA = 4, S_KB = S_KA + 2, S_KV = S_KB + 4, S_V = S_KV, S_FL = S_V + 10, S_CL = 24;
static_assert(S_ST + 4 <= S_KV || S_ST >= S_V + 7, "side jobs share registers");
constexpr bool w2t(int s) { return s >= W2T && !((s - W2T) & 1) && s < W2T + 14; }
static_assert(TW % 2 == 0, "the touches cover two whole steps");
constexpr bool wt(int s) { return s >= 0 && (s & ~1) + TW < 32; }
constexpr int extra(int s) {
  return wt(s) + w2t(s) + 3 * (s == 0) + (s == S_X0 || s == S_X1) + 3 * (s == S_KA) + 2 * (s == S_KB) + (s == S_ST + 3) +
         (s == S_V || s == S_V + 1) + (s == S_CL || s == S_CL + 1) + (s == S_V + 5 || s == S_V + 6) + (s == S_FL) + (s == CS0 - 1);
}
constexpr int L_CSG = 13 * 8192, L_CSP = 14 * 8192, L_CSF = L_CSP + 4096;
constexpr int C_TS = 32 + 4096, L_T = 143232;
constexpr int L_Z = L_T;
constexpr int C_US = 256 + 4096, L_U = L_T - 4 * C_US;
constexpr int CSPLIT = 20;
static_assert(L_CSG >= abuf(14) && L_T >= abuf(13) && L_T + 4 * C_TS <= L_ST && L_U >= 15 * 8192 && L_T >= 16 * 8192, "lds map");
constexpr int D_SLOT = 8192, N1 = 48, N2 = 16, NT = N1 + N2;
constexpr int PRE = 1, STR = 2, LND = 6, FHP = 29, WGR = 4;
constexpr int staged(int c) { return 2 * PRE + STR * c < 15 ? 2 * PRE + STR * c : 15; }
static_assert(DM0 + 12 < 20 && staged(CSPLIT) == 15 && staged(LND) >= 8, "");

template <int LO, int HI, class Hook>
__device__ __forceinline__ void conv_part(LAS char* tp, LAS char* gp, LAS char* zp, bool last, Hook&& hook) {
  constexpr int NH = HI / 2 + 1;
  s16x8 Fh[NH];
#pragma unroll
  for (int n = 0; n < NH; ++n) Fh[n] = cat8(tr_read(tp + n * 256), tr_read(tp + n * 256 - 32));
  WAIT_VL(0); BAR();
  unsigned ga = (unsigned)(size_t)gp, za = (unsigned)(size_t)zp;
  OPAQUE(ga); OPAQUE(za);
  s16x8 G[HI + 1];
#define G_RD(p) G[p] = cat8(tr_read((LAS char*)(size_t)(ga + (p) * 128)), tr_read((LAS char*)(size_t)(ga + (p) * 128 + 32)))
  const f32x4 zero = {0.f, 0.f, 0.f, 0.f};
  f32x4 z[2];
  float ta[4], tb[4], pr[4] = {0.f, 0.f, 0.f, 0.f}, u0, u1;
  auto keep = [&]() {
#pragma unroll
    for (int i = 0; i < 4; ++i) { ta[i] = z[0][i]; tb[i] = z[1][i]; }
  };
  if constexpr (LO == 0) { G_RD(0); G_RD(1); }
  else {
#pragma unroll
    for (int p = 1; p < LO; p += 2) G_RD(p);
    SCHED0();
    static_for<LO / 2>([&](auto J_) { constexpr int j = J_.value;
      z[j & 1] = mfma16(G[2 * j + 1], Fh[LO / 2 - 1 - j], j < 2 ? zero : z[j & 1]);
      G_RD(2 * j);
      SCHED0(); });
    G_RD(LO); G_RD(LO + 1);
    keep();
  }
  auto fin = [&](auto C_, auto K_) { constexpr int c = C_.value, k = K_.value, q = 3 - (k + 1) / 2;
    if constexpr (c < LO) {
      if constexpr (k == 0) { pr[3] = ta[3] + tb[3]; pr[2] = ta[2] + tb[2]; OPAQUE(pr[3]); OPAQUE(pr[2]); }
      if constexpr (k == 2) { pr[2] += dpp<0x13C>(pr[3]); pr[1] = ta[1] + tb[1]; OPAQUE(pr[2]); OPAQUE(pr[1]); }
      if constexpr (k == 4) { pr[1] += dpp<0x13C>(pr[2]); OPAQUE(pr[1]); }
    } else if constexpr (k == 6) {
      const unsigned o = pk_bf16(ta[0], tb[0]);
      *(LAS u16*)(size_t)(za + c * 512) = (u16)o;
      *(LAS u16*)(size_t)(za + c * 512 + 512) = (u16)(o >> 16);
    } else if constexpr (k & 1) {
      ta[q] += dpp<0x13C>(u0); tb[q] += dpp<0x13C>(u1); OPAQUE(ta[q]); OPAQUE(tb[q]);
    } else {
      u0 = last ? pr[q] : ta[q]; u1 = last ? ta[q] : tb[q]; pr[q] = tb[q]; OPAQUE(u0); OPAQUE(u1);
    }
  };
  SCHED0();
  static_for<(HI - LO + 1) / 2>([&](auto C_) {
    constexpr int c = LO + 2 * C_.value, nm = c + 2, g0 = nm > 10 ? 4 : nm > 7 ? nm - 7 : 0;
    hook(IC<c>{});
    static_for<nm>([&](auto J_) { constexpr int j = J_.value, n = c / 2 - j / 2;
      z[j & 1] = mfma16(G[c + (j & 1) - 2 * n], Fh[n], j < 2 ? zero : z[j & 1]);
      if constexpr (j < 2 && c + 2 <= HI) G_RD(c + 2 + j);
      if constexpr (c > 0) static_for<7>([&](auto K_) { if constexpr (j == (g0 + K_.value < nm ? g0 + K_.value : nm - 1)) fin(IC<c - 2>{}, K_); });
      if constexpr (j == nm / 2) hook(IC<c + 1>{});
      SCHED0(); });
    keep();
  });
  static_for<7>([&](auto K_) { fin(IC<HI - 1>{}, K_); });
}

__global__ __launch_bounds__(512, 2) void k_all(const u16* __restrict__ x, const u16* __restrict__ kk, const u16* __restrict__ vv,
                                                const u16* __restrict__ shk, const u16* __restrict__ ssmk, const u16* __restrict__ ssmd,
                                                const u16* __restrict__ w1, const u16* __restrict__ w3, const u16* __restrict__ w2,
                                                u16* __restrict__ out, char* __restrict__ ws) {
  extern __shared__ __attribute__((aligned(16))) char smem[];
  LAS char* sm = (LAS char*)smem;
  int tid = threadIdx.x, lane = tid & 63;
  const int wid = __builtin_amdgcn_readfirstlane(tid >> 6);
  const int bid = blockIdx.x, xcd = bid & 7, rr = bid >> 3;
  const int mb = (xcd & 1) * 4 + (rr & 3), nb = (xcd >> 1) * 8 + (rr >> 2);
  const int wm = wid & 3, wn = wid >> 2;
  int g = lane >> 4, fr = lane & 15, qq = fr >> 2, pp = lane & 3;
  const __amdgpu_buffer_rsrc_t rs = __builtin_amdgcn_make_buffer_rsrc(ws, 0, WS_END, 0x00020000);
  const unsigned lds0 = (unsigned)(size_t)sm;

  const char* gA[4];
#pragma unroll
  for (int i = 0; i < 4; ++i) gA[i] = (const char*)x + (size_t)(mb * 256 + i * 64 + wid * 8) * (D_ * 2);
  const char* gG = (const char*)w1 + ((size_t)(wid * 4) * F_ + nb * 112) * 2;
  const char* gU = (const char*)w3 + ((size_t)(wid * 4) * F_ + nb * 112) * 2;
  const unsigned vA = (lane >> 3) * (D_ * 2) + (((lane & 7) ^ ((wid & 1) * 4 + g)) * 16);
  const int tq = min(wid * 4 + (lane & 3), 27);
  const unsigned vB = lane < 56 ? (lane / 14) * (F_ * 2) + (lane % 14) * 16 : lane < 60 ? (tq / 14) * (F_ * 2) + (tq % 14) * 128 : (wid * 4 + (lane & 3)) * 2048;
  const unsigned wto = (TW * 32 + rr * 2) * (F_ * 2) + (xcd >> 1) * 1792;
  const unsigned lA = lds0 + wid * 1024, lB = lds0 + wid * U_GS;
#define A_DMA(q, i) dma16<((q) >> 1) * 128, abuf((q) >> 1) + (((q) & 1) * 2 + (i)) * 8192>(gA[((q) & 1) * 2 + (i)], vA, lA)
#define W_DMA(slot, i, NL) do { \
    if (i == 0) { dma16_m<(slot) * U_WS, NL>(gG, vB, lB); gG += 32 * F_ * 2; } \
    else { dma16_m<(slot) * U_WS + U_UP, NL>(gU, vB, lB); gU += 32 * F_ * 2; } } while (0)
#define G1_DMA() do { A_DMA(0, 0); A_DMA(0, 1); A_DMA(1, 0); A_DMA(1, 1); W_DMA(0, 0, 56); W_DMA(0, 1, 56); W_DMA(1, 0, 56); W_DMA(1, 1, 56); } while (0)
#define G2_DMA() do { A_DMA(2, 0); A_DMA(2, 1); A_DMA(3, 0); A_DMA(3, 1); W_DMA(2, 0, 56); W_DMA(2, 1, 56); } while (0)
  const int tb = bid >> 4, cb = bid & 15;
  f32x4 acc[4][7];
  u32x4 d[2], kr[5], vr[2];
  unsigned uo[2][4];
  const char* xrow = (const char*)x + (bid * 8 + wid) * (D_ * 2);
  G1_DMA();
#pragma unroll
  for (int i = 0; i < 28; ++i) { acc[i & 3][i >> 2] = (f32x4){0.f, 0.f, 0.f, 0.f}; OPAQUE(acc[i & 3][i >> 2]); }
  if (tid == 0) *(LAS u32x4*)(sm + L_CNT) = (u32x4){0u, 0u, 0u, 0u};
  unsigned ra[2][2], rw[2];
  ra[0][0] = lds0 + L_A + (wm * 64 + fr) * 128 + ((g ^ (fr >> 1)) * 16);
  ra[0][1] = ra[0][0] ^ 64;
  ra[1][0] = ra[0][0] + 65536; ra[1][1] = ra[0][1] + 65536;
  rw[0] = lds0 + 2 * g * U_GS + qq * 224 + wn * 112 + pp * 8;
  rw[1] = lds0 + 2 * g * U_GS + qq * 224 + wn * 112 + 96 + (pp & 1) * 8 + (pp >> 1) * U_UP;
  OPAQUE(ra[0][0]); OPAQUE(ra[0][1]); OPAQUE(ra[1][0]); OPAQUE(ra[1][1]); OPAQUE(rw[0]); OPAQUE(rw[1]);
  SCHED0();
  G2_DMA();
#define U_RD(i, X, Wl, Wh, t) do { \
    if constexpr (i >= 2 && i < 6) X[i - 2] = *(LAS s16x8*)(size_t)(ra[((t) >> 1) % 3 == 1][(t) & 1] + (((t) >> 1) % 3 == 2 ? 32768 : 0) + (i - 2) * 2048); \
    else { constexpr int j_ = i < 2 ? i : i - 4, c_ = j_ >> 1; \
      LAS char* p_ = (LAS char*)(size_t)(rw[c_ == 6] + ((t) & 3) * U_WS + (c_ == 6 ? 0 : (c_ >> 1) * 32 + (c_ & 1) * U_UP) + (j_ & 1) * U_GS); \
      if constexpr (j_ & 1) Wh[c_] = tr_read(p_); else Wl[c_] = tr_read(p_); } } while (0)
  float cs[7] = {0.f, 0.f, 0.f, 0.f, 0.f, 0.f, 0.f};
  s16x8 X[2][4];
  s16x4 Wl[2][7], Wh[2][7];
  WAIT_V(8); BAR();
  static_for<18>([&](auto I_) { constexpr int i = I_.value; U_RD(i, X[0], Wl[0], Wh[0], 0); });
  unsigned ca = lds0 + wn * U_UP + mb * U_GS + g * 224 + wm * 56 + fr * 4;
  OPAQUE(ca);
  unsigned cw = *(LAS unsigned*)(size_t)ca;
  float c0 = 0.f, c1 = 0.f;
  SCHED0(); WAIT_VL(6); BAR(); SCHED0();

  const int cch = wid & 3;
  unsigned tag = 0, fu = 0, dsk = 0, ep;
  u32x2 skr;
  u32x4 sg, cg[2];
#define U_DMA(s, i) do { \
    if constexpr ((s) < 28 && (i) < 2) W_DMA((s) & 3, (i), 56); \
    if constexpr ((s) < 27 && (i) >= 2) A_DMA((s) + 5, (i) - 2); \
    if constexpr ((s) == 27 && (i) >= 2) { OPAQUE(tid); const unsigned l_ = tid & 63; \
      dma16_s<((i) - 2) * 1024, ((i) - 2) * 1024 + L_T + 32>((const char*)ssmk + (size_t)(bid * 4 + cch) * 4096 + wn * 2048, l_ * 16, lds0 + cch * C_TS + wn * 2048); } } while (0)
  float ax, ay, bx, by, px, py, qx, qy, mean;
  f32x4 tx, ty;
#define PIN(a, b) do { OPAQUE(a); OPAQUE(b); } while (0)
#define UP2(a, b, v) do { a = bflo(v); b = bfhi(v); PIN(a, b); } while (0)
  auto side = [&](auto S_c, auto I_) { constexpr int s = S_c.value, i = I_.value;
    constexpr bool slot = !(i & 1);
    if constexpr (s == 4) {
      if constexpr (i == 0) { LANDED(ep); LANDED(skr); tag = __builtin_amdgcn_readfirstlane(ep) + 0x9E3779B9u; }
      if constexpr (i == 3) UP2(tx[0], tx[1], skr[0]);
      if constexpr (i == 4) UP2(tx[2], tx[3], skr[1]);
      if constexpr (i == 8) { OPAQUE(tid); *(LAS f32x4*)(sm + L_SK + (tid & 63) * 16) = tx; }
    }
    if constexpr (slot && s >= S_ST && s < S_ST + 4) {
      constexpr int n = (s - S_ST) * 14 + (i >> 1), r = n < 22 ? n - 16 : n - 46;
      if constexpr (n == 0) { LANDED(d[0]); LANDED(d[1]); }
      if constexpr (n == 22) PIN(d[0], d[1]);
      if constexpr (n < 16) {
        if constexpr (!(n & 1)) UP2(ax, ay, d[n >> 3][(n >> 1) & 3]);
        else { if constexpr (n == 1) { px = ax; py = ay; } else { px += ax; py += ay; } PIN(px, py); }
      }
      if constexpr (n >= 22 && n < 46) {
        constexpr int j = (n - 22) / 3, k = (n - 22) % 3;
        if constexpr (k == 0) UP2(ax, ay, d[j >> 2][j & 3]);
        if constexpr (k == 1) { ax -= mean; ay -= mean; PIN(ax, ay); }
        if constexpr (k == 2) { px = j ? fmaf(ax, ax, px) : ax * ax; py = j ? fmaf(ay, ay, py) : ay * ay; PIN(px, py); }
      }
      if constexpr (r == 0) { px += py; px += dpp<0xB1>(px); OPAQUE(px); }
      if constexpr (r == 1) { px += dpp<0x4E>(px); OPAQUE(px); }
      if constexpr (r == 2) { px += dpp<0x141>(px); OPAQUE(px); }
      if constexpr (r == 3) { px += dpp<0x140>(px); OPAQUE(px); }
      if constexpr (r == 4) { px = add16(px, px); OPAQUE(px); }
      if constexpr (r == 5) { px = add32(px, px) * (1.0f / D_); OPAQUE(px); if constexpr (n < 22) mean = px; }
      if constexpr (r == 6) { px = rbf(px) + 1e-5f; OPAQUE(px); }
      if constexpr (r == 7) { px = __builtin_amdgcn_sqrtf(rbf(px)); OPAQUE(px); }
      if constexpr (r == 8) { px = rbf(px); mean = rbf(mean); PIN(px, mean); }
      if constexpr (r == 9) {
        OPAQUE(tid);
        ST_SC1(((u32x4){__float_as_uint(mean), tag, __float_as_uint(px), tag}), WS_ST + (bid * 8 + wid) * 16 + (tid & 63) * 0x2000000);
      }
    }
    if constexpr (i == 27 && s >= S_KV - 1 && s < S_KV + 3) {
      OPAQUE(tid);
      tx = *(LAS f32x4*)(sm + L_SK + (tid & 7) * 128 + (s + 1 - S_KV) * 32);
      ty = *(LAS f32x4*)(sm + L_SK + (tid & 7) * 128 + (s + 1 - S_KV) * 32 + 16);
    }
    if constexpr (slot && s >= S_KV && s < S_KV + 4) {
      constexpr int e = s - S_KV, k = i >> 1;
      if constexpr (e == 0 && k == 0) { LANDED(kr[0]); LANDED(kr[1]); LANDED(kr[2]); LANDED(kr[3]); LANDED(kr[4]); }
      if constexpr (k == 0) UP2(ax, ay, kr[4][e]);
      if constexpr (k == 1) UP2(bx, by, kr[3][e]);
      if constexpr (k == 2) { qx = ax * tx[0]; qy = ay * ty[0]; PIN(qx, qy); }
      if constexpr (k == 3) { px = bx * tx[0]; py = by * ty[0]; PIN(px, py); }
      if constexpr (k == 4) UP2(ax, ay, kr[2][e]);
      if constexpr (k == 5) { qx = fmaf(bx, tx[1], qx); qy = fmaf(by, ty[1], qy); PIN(qx, qy); }
      if constexpr (k == 6) { px = fmaf(ax, tx[1], px); py = fmaf(ay, ty[1], py); PIN(px, py); }
      if constexpr (k == 7) UP2(bx, by, kr[1][e]);
      if constexpr (k == 8) { qx = fmaf(ax, tx[2], qx); qy = fmaf(ay, ty[2], qy); PIN(qx, qy); }
      if constexpr (k == 9) { px = fmaf(bx, tx[2], px); py = fmaf(by, ty[2], py); PIN(px, py); }
      if constexpr (k == 10) UP2(ax, ay, kr[0][e]);
      if constexpr (k == 11) { qx = fmaf(bx, tx[3], qx); qy = fmaf(by, ty[3], qy); PIN(qx, qy); }
      if constexpr (k == 12) { px = fmaf(ax, tx[3], px); py = fmaf(ay, ty[3], py); PIN(px, py); }
      if constexpr (k == 13) { uo[0][e] = pk_bf16(px, py); uo[1][e] = pk_bf16(qx, qy); PIN(uo[0][e], uo[1][e]); }
    }
    if constexpr (slot && s >= S_V + 5 && s < S_V + 7) {
      constexpr int e = (s - S_V - 5) * 2 + (i >> 1) / 7, k = (i >> 1) % 7;
      if constexpr (e == 0 && k == 0) { LANDED(vr[0]); LANDED(vr[1]); }
      if constexpr (k == 0) UP2(ax, ay, uo[0][e]);
      if constexpr (k == 1) UP2(px, py, vr[0][e]);
      if constexpr (k == 2) UP2(bx, by, uo[1][e]);
      if constexpr (k == 3) UP2(qx, qy, vr[1][e]);
      if constexpr (k == 4) { px *= ax; py *= ay; PIN(px, py); }
      if constexpr (k == 5) { qx *= bx; qy *= by; PIN(qx, qy); }
      if constexpr (k == 6) { uo[0][e] = pk_bf16(px, py); uo[1][e] = pk_bf16(qx, qy); PIN(uo[0][e], uo[1][e]); }
      if constexpr (i == 26) {
        OPAQUE(tid);
        __builtin_amdgcn_raw_buffer_store_b128((u32x4){uo[0][e - 1], uo[0][e], uo[1][e - 1], uo[1][e]}, rs, (tid & 7) * (2 * S_ * 8) + (tid >> 3) * 16,
                                               WS_U + ((cb * 16 + (e >> 1)) * S_ + tb * 128) * 8, 16);
      }
    }
    if constexpr (i == 20) {
      if constexpr (wt(s)) touch_hi<56>((const char*)(s & 1 ? w3 : w1) + wto + (s & ~1) * 32 * (F_ * 2), vB, lds0 + L_JUNK);
      if constexpr (s == S_CL || s == S_CL + 1) { OPAQUE(tid); __builtin_amdgcn_raw_buffer_store_b128((u32x4){~0u, ~0u, ~0u, ~0u}, rs, tid * 16, WS_EX + bid * 16384 + (s - S_CL) * 8192, 16); }
      if constexpr (s == 0) {
        const char* pc;
        asm volatile("s_getpc_b64 %0" : "=s"(pc));
        touch<2>(pc + min(rr * 8 + wid, CODE_BYTES / 256 - 1) * 256, vB, lds0 + L_JUNK);
        OPAQUE(tid);
        asm volatile("global_load_dwordx2 %0, %1, %2 ; HID %0" : "=&v"(skr) : "v"((tid & 63) * 8), "s"((const char*)shk + cb * 512));
        asm volatile("v_mov_b32 %0, 0\n\tglobal_load_dword %0, %0, %1 sc1 ; HID %0" : "=&v"(ep) : "s"(ws + WS_EP));
      }
      if constexpr (s == S_X0 || s == S_X1) { OPAQUE(tid); LD_HID(d[s - S_X0], (tid & 63) * 16, xrow, (s - S_X0) * 1024, ""); }
      if constexpr (s == S_KA || s == S_KB) {
        OPAQUE(tid);
        const __amdgpu_buffer_rsrc_t rk = __builtin_amdgcn_make_buffer_rsrc((void*)kk, 0, S_ * D_ * 2, 0x00020000);
        const unsigned o = (tid >> 3) * (D_ * 4) + (tid & 7) * 16 + (tb * 128 - 3) * (D_ * 2) + cb * 128;
        if constexpr (s == S_KA) { LD_HIDB(kr[4], o + 4 * D_ * 2, rk, " nt"); LD_HIDB(kr[2], o + 2 * D_ * 2, rk, " nt"); LD_HIDB(kr[0], o, rk, " nt"); }
        else { LD_HIDB(kr[3], o + 3 * D_ * 2, rk, " nt"); LD_HIDB(kr[1], o + D_ * 2, rk, " nt"); }
      }
      if constexpr (s == S_V || s == S_V + 1) {
        OPAQUE(tid);
        LD_HID(vr[s - S_V], (tid >> 3) * (D_ * 4) + (tid & 7) * 16, (const char*)vv + tb * 128 * (D_ * 2) + cb * 128, (s - S_V) * (D_ * 2), " nt");
      }
      if constexpr (s == S_FL) { OPAQUE(tid); __builtin_amdgcn_raw_buffer_store_b32(tag, rs, WS_FU + (cb * 16 + tb) * 128 + min(tid, 1) * 0x2000000, 0, 16); }
    }
    if constexpr (i == 24) {
      if constexpr (w2t(s)) touch_hi<60>((const char*)w2 + ((s - W2T) / 2 * 16 + (bid >> 4)) * (32 * D_ * 2) + (bid & 15) * 128, vB, lds0 + L_JUNK);
    }
  };
  static_for<31>([&](auto S_c) {
    constexpr int s = S_c.value, b = s & 1;
    if constexpr (s == 28) {
      OPAQUE(tid);
      sg = LD_SC1(WS_ST + (mb * 256 + (tid & 255)) * 16);
      fu = __hip_atomic_load((GAS unsigned*)(ws + WS_FU + ((bid >> 4) * 16 + (tid & 15)) * 128), RLX);
      dsk = __hip_atomic_load((GAS u16*)(ssmd + bid * 4 + (tid & 3)), RLX);
    }
    if constexpr (s == 30) {
      OPAQUE(tid);
#pragma unroll
      for (int k = 0; k < 2; ++k) cg[k] = LD_SC1(WS_CS + nb * 16384 + k * 8192 + tid * 16);
    }
    SCHED0();
    static_for<28>([&](auto I_) { constexpr int i = I_.value, c = i >> 2, m = i & 3;
      mfma_acc(acc[m][c], cat8(Wl[b][c], Wh[b][c]), X[b][m]);
      if constexpr (i < 18) U_RD(i, X[b ^ 1], Wl[b ^ 1], Wh[b ^ 1], s + 1);
      constexpr int dj = i - (s ? DM0 : 5);
      if constexpr (dj >= 0 && dj % 4 == 0 && dj < 16) U_DMA(s, dj / 4);
      if constexpr (s == 0 && i < 6 && i % 3 != 2) { if constexpr (i < 2) W_DMA(3, i, 56); else A_DMA(4, i - 3); }
      side(S_c, I_);
      if constexpr (s < CS0) {
        if constexpr (i == 1) { c0 += bflo(cw); OPAQUE(c0); }
        if constexpr (i == 3) { c1 += bfhi(cw); OPAQUE(c1); }
        if constexpr (i == 18 && s < CS0 - 1) cw = *(LAS unsigned*)(size_t)(ca + ((s + 1) & 3) * U_WS);
        if constexpr (s == CS0 - 1) {
          if constexpr (i == 5) { c0 = add32(c0, c1); OPAQUE(c0); }
          if constexpr (i == 9) { c0 = add16(c0, c0); OPAQUE(c0); }
          if constexpr (i == 21) {
            OPAQUE(tid);
            __builtin_amdgcn_raw_buffer_store_b64((u32x2){__float_as_uint(c0), tag}, rs, (tid & 15) * 16 + (tid & 32) / 4 + (((tid & 15) + 2) >> 4) * 0x2000000,
                                                  WS_CS + (nb * 8 + mb) * 2048 + wn * 1024 + wm * 224, 16);
          }
        }
      }
      if constexpr ((m == 1 || m == 2) && s >= CS0) colsum<m - 1>(cs[c], cat8(Wl[b][c], Wh[b][c]), wm);
      if constexpr (s == 30 && i >= 14) colsum<i & 1>(cs[(i - 14) >> 1], cat8(Wl[1][(i - 14) >> 1], Wh[1][(i - 14) >> 1]), wm);
      SCHED0(); });
    if constexpr (s < 30) {
      WAIT_VL((s == 28 ? 7 : s == 29 ? 5 : s == 1 ? 10 : s < 4 ? 8 : 7) + (s < 4 || s > 27) * extra(s - 2) + extra(s - 1) + extra(s));
      BAR(); SCHED0();
    }
  });
  OPAQUE(tid); lane = tid & 63; g = lane >> 4; fr = lane & 15; qq = fr >> 2; pp = lane & 3;
  const int mb2 = mb * 2 + ((rr >> 2) & 1);
  const int kh = xcd >> 2, nb2 = ((xcd >> 1) & 1) * 4 + (rr >> 3), pbid = bid ^ 4;

  const char* wB = (const char*)w2 + ((size_t)(cch * 8) * D_ + nb2 * 128) * 2;
  const char* gB = wB + (size_t)(kh * NT + wn * PRE) * (32 * D_ * 2);
  const unsigned v2 = g * (D_ * 2) + ((fr ^ ((g | ((wid & 1) << 2)) << 1)) * 16);
  const unsigned dB = lds0 + cch * 2048;
#define D_STAGE(d, o) do { dma16<0, (o)>(gB, v2, d); dma16<0, (o) + 1024>(gB + 4 * D_ * 2, v2, d); gB += 32 * D_ * 2; } while (0)
  u32x4 ur[2];
  f32x4 cv[7];
  f32x2 st[4];
  {
    float r[8], r2[8];
    SCHED0();
    static_for<28>([&](auto I_) { constexpr int i = I_.value, c = i >> 2, m = i & 3;
      mfma_acc(acc[m][c], cat8(Wl[1][c], Wh[1][c]), X[1][m]);
      if constexpr (i == 1) {
        *(LAS float*)(sm + L_CSP + wid * 512 + lane * 4) = add16(add32(cs[0], cs[1]), add32(cs[2], cs[3]));
        *(LAS float*)(sm + L_CSP + wid * 512 + 256 + lane * 4) = add16(add32(cs[4], cs[5]), add32(cs[6], 0.f));
      }
      if constexpr (i == 4) {
        for (unsigned spins = 0; !__all(sg[1] == tag && sg[3] == tag) && spins < SPIN_CAP; ++spins) {
          __builtin_amdgcn_s_sleep(2); FENCE_C();
          sg = LD_SC1(WS_ST + (mb * 256 + (tid & 255)) * 16);
        }
        *(LAS u32x2*)(sm + L_ST + (tid & 255) * 8) = (u32x2){sg[0], sg[2]};
        wait_flags((GAS unsigned*)(ws + WS_FU + ((bid >> 4) * 16 + fr) * 128), fu, tag);
#pragma unroll
        for (int k = 0; k < 2; ++k) ur[k] = __builtin_amdgcn_raw_buffer_load_b128(rs, WS_U + bid * 16384 + tid * 32 + k * 16, 0, 16);
      }
      if constexpr (i == 3) {
        for (unsigned spins = 0; !__all(lane >= 56 || (cg[0][1] == tag && cg[0][3] == tag && cg[1][1] == tag && cg[1][3] == tag)) && spins < SPIN_CAP; ++spins) {
          __builtin_amdgcn_s_sleep(2); FENCE_C();
#pragma unroll
          for (int k = 0; k < 2; ++k) cg[k] = LD_SC1(WS_CS + nb * 16384 + k * 8192 + tid * 16);
        }
        *(LAS f32x2*)(sm + L_CSG + tid * 8) = (f32x2){__uint_as_float(cg[0][0]) + __uint_as_float(cg[1][0]), __uint_as_float(cg[0][2]) + __uint_as_float(cg[1][2])};
      }
      if constexpr (i == 6) {
        if (wid < 4 && lane == wid) {
          LAS char* p = sm + L_T + wid * C_TS;
          *(LAS u32x4*)p = (u32x4){0u, 0u, 0u, 0u}; *(LAS u32x4*)(p + 16) = (u32x4){0u, 0u, 0u, 0u};
          *(LAS u16*)(p + 32) = (u16)pk_bf16(bflo(*(LAS u16*)(p + 32)) + bflo(dsk), 0.f);
        }
      }
      if constexpr (i == 10) {
        WAIT_L0(); BAR();
#pragma unroll
        for (int k = 0; k < 8; ++k) r[k] = *(LAS float*)(sm + L_CSP + wn * 2048 + k * 256 + lane * 4);
#pragma unroll
        for (int k = 0; k < 4; ++k) st[k] = *(LAS f32x2*)(sm + L_ST + (wm * 64 + k * 16 + fr) * 8);
        LAS char* q = sm + L_CSG + wn * 224;
#pragma unroll
        for (int k = 0; k < 4; ++k) {
          r2[2 * k] = *(LAS float*)(q + k * 1024 + (g >> 1) * 512 + (lane & 31) * 4);
          r2[2 * k + 1] = *(LAS float*)(q + k * 1024 + 128 + (g & 1 ? (fr >> 3) * 512 + 64 + (fr & 7) * 4 : (g >> 1) * 512 + fr * 4));
        }
      }
      if constexpr (i == 16) {
        LAS char* q = sm + L_CSF + wid * 512;
        *(LAS float*)(q + (((g & 1) << 1) | (g >> 1)) * 64 + fr * 4) = ((r[0] + r[2]) + (r[4] + r[6])) + ((r2[0] + r2[2]) + (r2[4] + r2[6]));
        *(LAS float*)(q + (((g & 1) << 1) | (g >> 1)) * 64 + 256 + fr * 4) = ((r[1] + r[3]) + (r[5] + r[7])) + ((r2[1] + r2[3]) + (r2[5] + r2[7]));
#pragma unroll
        for (int c2 = 0; c2 < 7; ++c2) cv[c2] = *(LAS f32x4*)(q + c2 * 64 + g * 16);
      }
      if constexpr (i == 23) D_STAGE(dB + wn * (PRE * D_SLOT), 0);
      if constexpr (i == 20) {
        if (tid < 64) *(LAS u32x4*)(sm + L_U + g * C_US + fr * 16) = (u32x4){0u, 0u, 0u, 0u};
      }
      SCHED0(); });
  }
  MFMA_DONE();

  {
    float ea[4], s2[4];
#pragma unroll
    for (int m = 0; m < 4; ++m) { ea[m] = -1.44269504089f * __builtin_amdgcn_rcpf(st[m][1]); s2[m] = st[m][1] * st[m][1]; }
    auto sw = [](f32x2 a, f32x2 b, float e, float q) {
      const f32x2 t = a * e;
      const f32x2 d = (f32x2){__builtin_amdgcn_exp2f(t[0]), __builtin_amdgcn_exp2f(t[1])} * q + q;
      const f32x2 h = a * b * (f32x2){__builtin_amdgcn_rcpf(d[0]), __builtin_amdgcn_rcpf(d[1])};
      return pk_bf16(h[0], h[1]);
    };
    const unsigned hb = WS_H + ((nb * 14 + wn * 7) * S_ + mb * 256 + wm * 64) * 16;
    const unsigned ho = ((g >> 1) * S_ + (g & 1) * 16 + fr) * 16;
#pragma unroll
    for (int P = 0; P < 3; ++P)
#pragma unroll
      for (int mp = 0; mp < 2; ++mp) {
        u32x2 h[2];
#pragma unroll
        for (int i = 0; i < 2; ++i) {
          const int m = 2 * mp + i;
          const f32x4 ga = acc[m][2 * P] - st[m][0] * cv[2 * P], up = acc[m][2 * P + 1] - st[m][0] * cv[2 * P + 1];
          h[i] = (u32x2){sw(ga.xy, up.xy, ea[m], s2[m]), sw(ga.zw, up.zw, ea[m], s2[m])};
        }
        ST_SC1(swap16(h[0], h[1]), hb + ho + (2 * P * S_ + mp * 32) * 16);
        SCHED0();
      }
    u32x2 t6[2];
#pragma unroll
    for (int mp = 0; mp < 2; ++mp) {
      const float e = lane < 32 ? ea[2 * mp] : ea[2 * mp + 1], q = lane < 32 ? s2[2 * mp] : s2[2 * mp + 1];
      f32x4 ga = acc[2 * mp][6] - st[2 * mp][0] * cv[6], up = acc[2 * mp + 1][6] - st[2 * mp + 1][0] * cv[6];
#pragma unroll
      for (int j = 0; j < 4; ++j) {
        auto r = __builtin_amdgcn_permlane32_swap(__float_as_uint(ga[j]), __float_as_uint(up[j]), false, false);
        ga[j] = __uint_as_float(r[0]); up[j] = __uint_as_float(r[1]);
      }
      t6[mp] = (u32x2){sw(ga.xy, up.xy, e, q), sw(ga.zw, up.zw, e, q)};
    }
    ST_SC1(swap16(t6[0], t6[1]), hb + (6 * S_ + ((g & 1) * 2 + (g >> 1)) * 16 + fr) * 16);
  }
#pragma unroll
  for (int c = 0; c < 4; ++c) {
    const unsigned sel = (c & 1) ? 0x07060302u : 0x05040100u;
    *(LAS u32x2*)(sm + L_U + c * C_US + 256 + tid * 8) =
        (u32x2){__builtin_amdgcn_perm(ur[0][2 + (c >> 1)], ur[0][c >> 1], sel), __builtin_amdgcn_perm(ur[1][2 + (c >> 1)], ur[1][c >> 1], sel)};
  }

  LAS char* tp = sm + L_T + cch * C_TS + 32 + (-12 + 4 * pp + 4 * (31 - 8 * g - qq)) * 2;
  LAS char* gp = sm + L_U + cch * C_US + 256 + (16 * pp - 112 + 4 * (8 * g + qq)) * 2;
  LAS char* zp = sm + L_Z + lane * 8 + cch * 2;
  if (wid < 4) {
    gB = wB + (size_t)(kh * NT + 2 * PRE) * (32 * D_ * 2);
#define D_GROUP(j) do { const unsigned d_ = dB + (((j) + 3) & 3) * (4 * D_SLOT);     \
      D_STAGE(dB + (((j) + 2) & 3) * (4 * D_SLOT), 3 * D_SLOT); \
      if ((j) + 3 == N1 / 4) gB = wB + (size_t)N1 * (32 * D_ * 2); \
      D_STAGE(d_, 0); D_STAGE(d_, D_SLOT); D_STAGE(d_, 2 * D_SLOT); } while (0)
    conv_part<0, CSPLIT - 1>(tp, gp, zp, lane == 63, [&](auto P_) { constexpr int p = P_.value;
      if constexpr (p == 0) __builtin_amdgcn_raw_buffer_store_b32(tag, rs, WS_FH + (mb * 32 + nb) * 128 + min(tid, 1) * 0x2000000, 0, 16);
      if constexpr (p == LND) { WAIT_V(2 * (staged(p) - 8)); if (lane == 0) lds_add(sm + L_CNT); }
      static_for<staged(p + 1) - staged(p)>([&](auto I_) { D_STAGE(dB, (staged(p) + I_.value) * D_SLOT); }); });
    FENCE_C();
    if (lane == 0) lds_add(sm + L_CNT + 8);
    lds_wait(sm + L_CNT + 8, 8);
    D_STAGE(dB, 15 * D_SLOT);
    {
      u32x4 o[4];
#pragma unroll
      for (int i = 0; i < 4; ++i) o[i] = *(LAS u32x4*)(sm + L_Z + i * 4096 + tid * 16);
#pragma unroll
      for (int i = 0; i < 4; ++i) ST_SC1(o[i], WS_Z + bid * 16384 + i * 4096 + tid * 16);
    }
    WAIT_V(6); BAR();
    static_for<7>([&](auto I_) { D_STAGE(dB, I_.value * D_SLOT); });
    WAIT_V(18); BAR();
    D_GROUP(3);
    for (int j = 4; j < NT / 4 - 3; ++j) {
      WAIT_V(14); BAR();
      if (j == 4 && tid == 0) __hip_atomic_store((GAS unsigned*)(ws + WS_FZ + bid * 128), tag, RLX);
      D_GROUP(j);
    }
    WAIT_V(14); BAR();
    D_STAGE(dB, ((NT - 1) & 15) * D_SLOT);
    WAIT_V(8); BAR();
    WAIT_V(0); BAR();
    if (bid == 0 && wid == 3) {
#pragma unroll
      for (int i = 0; i < 4; ++i) { GAS unsigned* fp = (GAS unsigned*)(ws + WS_FU + (i * 64 + lane) * 128); wait_flags(fp, __hip_atomic_load(fp, RLX), tag); }
      if (lane == 0) __hip_atomic_store((GAS unsigned*)(ws + WS_EP), tag, RLX);
    }
    return;
  }

  __builtin_amdgcn_s_setprio(1);
  unsigned fh;
  conv_part<CSPLIT, 31>(tp, gp, zp, lane == 63, [&](auto P_) {
    if constexpr (P_.value == FHP) {
      OPAQUE(tid); const int l = tid & 63;
      fh = __builtin_amdgcn_raw_buffer_load_b32(rs, (l < 19 ? kh * 13 + l : nb ^ 16) * 128, WS_FH + mb * 32 * 128, 16);
    } });
  FENCE_C();
  if (lane == 0) lds_add(sm + L_CNT + 8);
  const int row0 = mb2 * 128 + kh * 64 + cch * 16 + fr;
  u32x4 xv[4];
  {
    GAS unsigned* fp = (GAS unsigned*)(ws + (lane < 19 ? WS_FH + (mb * 32 + kh * 13 + lane) * 128 : WS_FH + (mb * 32 + (nb ^ 16)) * 128));
    wait_flags(fp, fh, tag);
  }
  const unsigned vK = WS_H + (g * S_ + row0) * 16, vS = vK ^ 1024;
  unsigned ko = kh * NT * (4 * S_ * 16);
  u32x4 Hf[8][2];
#define H_LD(t, m) Hf[(t) & 7][m] = __builtin_amdgcn_raw_buffer_load_b128(rs, (m) ? vS : vK, ko, 16)
  static_for<8>([&](auto I_) { H_LD(I_.value, 0); H_LD(I_.value, 1); ko += 4 * S_ * 16; });
  unsigned rb[2][8];
#pragma unroll
  for (int c = 0; c < 8; ++c) {
    rb[0][c] = lds0 + (8 * g + qq) * 256 + ((c ^ (qq | ((g & 1) << 2))) * 32) + pp * 8;
    rb[1][c] = rb[0][c] + 8 * D_SLOT; OPAQUE(rb[0][c]); OPAQUE(rb[1][c]);
  }
  s16x4 Vl[2][8], Vh[2][8];
#define D_RD(i, b, slot) do { LAS char* p_ = (LAS char*)(size_t)(rb[((slot) >> 3) & 1][(i) >> 1] + ((slot) & 7) * D_SLOT + ((i) & 1) * 1024); \
    if constexpr ((i) & 1) Vh[b][(i) >> 1] = tr_read(p_); else Vl[b][(i) >> 1] = tr_read(p_); } while (0)
#define D_STEP(s, NM, LD, RD, FI) do { constexpr int b_ = (s) & 1; \
    if constexpr ((s) == 3 && (FI)) { asm volatile("s_cmp_eq_u32 %0, 0\n\ts_cbranch_scc1 1f\n\ts_barrier\n1:" ::"s"(it) : "scc", "memory"); } \
    else if constexpr (((s) & 3) == 3 && RD) BAR(); \
    SCHED0(); \
    static_for<8 * NM>([&](auto I_) { constexpr int i = I_.value, c = NM == 2 ? i >> 1 : i, m = NM == 2 ? i & 1 : 0; \
      if constexpr (c % WGR == 0 && m == 0 && RD) __builtin_amdgcn_s_waitcnt(0xC07F | (16 - 2 * WGR) << 8); \
      mfma_acc(ac[m][c], cat8(Vl[b_][c], Vh[b_][c]), __builtin_bit_cast(s16x8, Hf[(s) & 7][m])); \
      if constexpr (RD) { if constexpr (NM == 2) D_RD(i, b_ ^ 1, (s) + 1); else { D_RD(2 * i, b_ ^ 1, (s) + 1); D_RD(2 * i + 1, b_ ^ 1, (s) + 1); } } \
      if constexpr (c == 7 && m < LD) H_LD(s, m); \
      SCHED0(); }); \
    ko += 4 * S_ * 16; } while (0)
  f32x4 ac[2][8];
#pragma unroll
  for (int i = 0; i < 16; ++i) { ac[i >> 3][i & 7] = (f32x4){0.f, 0.f, 0.f, 0.f}; OPAQUE(ac[i >> 3][i & 7]); }
  GAS unsigned* fzp = (GAS unsigned*)(ws + WS_FZ + (nb2 * 32 + (lane & 31)) * 128);
  u32x2 zv[8];
  lds_wait(sm + L_CNT, 4);
  static_for<16>([&](auto I_) { D_RD(I_.value, 0, 0); });
  int it;
  for (it = 0; it < N1 / 16 - 1; ++it) static_for<16>([&](auto J_) { D_STEP(J_.value, 2, 2, true, true); });
  unsigned fz = __hip_atomic_load(fzp, RLX);
  static_for<16>([&](auto J_) { constexpr int s = J_.value;
    if constexpr (s == 8) {
      ko = N1 * (4 * S_ * 16);
      wait_flags(fzp, fz, tag);
#pragma unroll
      for (int c = 0; c < 8; ++c) zv[c] = __builtin_amdgcn_raw_buffer_load_b64(rs, WS_Z + ((nb2 * 32 + c * 4 + g) * S_ + row0) * 8, 0, 16);
    }
    D_STEP(s, 2, (s < 8 ? 2 : 1), true, false); });
  MFMA_DONE();
#pragma unroll
  for (int P = 0; P < 4; ++P) {
    u32x4 o;
#pragma unroll
    for (int i = 0; i < 4; ++i) {
      const f32x4 a = ac[1][2 * P + (i >> 1)];
      o[i] = min(pk_bf16(a[(i & 1) * 2], a[(i & 1) * 2 + 1]), 0xfffffffeu);
    }
    ST_SC1(o, WS_EX + pbid * 16384 + P * 4096 + (tid & 255) * 16);
  }
  u32x4 pv[4];
  static_for<N2>([&](auto J_) { constexpr int s = J_.value;
    if constexpr (s == N2 - 8) {
#pragma unroll
      for (int P = 0; P < 4; ++P) xv[P] = *(const u32x4*)(x + (size_t)row0 * D_ + nb2 * 128 + (2 * P + (g & 1)) * 16 + (g >> 1) * 8);
    }
    if constexpr (s == N2 - 3) {
#pragma unroll
      for (int i = 0; i < 4; ++i) pv[i] = LD_SC1(WS_EX + bid * 16384 + i * 4096 + (tid & 255) * 16);
    }
    D_STEP(s, 1, (s + 8 < N2 ? 1 : 0), (s + 1 < N2), false);
  });
  MFMA_DONE();
  for (unsigned spins = 0; spins < SPIN_CAP; ++spins) {
    unsigned mx = 0;
#pragma unroll
    for (int i = 0; i < 16; ++i) mx = max(mx, pv[i >> 2][i & 3]);
    if (!__any(mx == ~0u)) break;
    __builtin_amdgcn_s_sleep(1); FENCE_C();
#pragma unroll
    for (int i = 0; i < 4; ++i) pv[i] = LD_SC1(WS_EX + bid * 16384 + i * 4096 + (tid & 255) * 16);
  }
  {
    const f32x2 sv = *(LAS f32x2*)(sm + L_ST + (row0 & 255) * 8);
    const float ri = __builtin_amdgcn_rcpf(sv[1]), mo = -sv[0] * ri;
    const __amdgpu_buffer_rsrc_t ro = __builtin_amdgcn_make_buffer_rsrc(out, 0, S_ * D_ * 2, 0x00020000);
#pragma unroll
    for (int P = 0; P < 4; ++P) {
      const u32x4 xs = swap16((u32x2){xv[P][0], xv[P][1]}, (u32x2){xv[P][2], xv[P][3]});
      u32x2 o[2];
#pragma unroll
      for (int i = 0; i < 4; ++i) {
        const int c = 2 * P + (i >> 1), d = i & 1;
        const float x0 = bflo(xs[i]), x1 = bfhi(xs[i]);
        const float y0 = fmaf(fmaf(x0, ri, mo), bflo(zv[c][d]), x0) + (ac[0][c][2 * d] + bflo(pv[P][i]));
        const float y1 = fmaf(fmaf(x1, ri, mo), bfhi(zv[c][d]), x1) + (ac[0][c][2 * d + 1] + bfhi(pv[P][i]));
        o[i >> 1][d] = pk_bf16(y0, y1);
      }
      __builtin_amdgcn_raw_buffer_store_b128(swap16(o[0], o[1]), ro, (row0 * D_ + nb2 * 128 + (2 * P + (g & 1)) * 16 + (g >> 1) * 8) * 2, 0, 16);
    }
  }
  WAIT_V0C();
}

extern "C" void kernel_launch(void* const* d_in, const int* in_sizes, int n_in, void* d_out, int out_size, void* d_ws, size_t ws_size,
                              hipStream_t stream) {
  (void)hipFuncSetAttribute((const void*)k_all, hipFuncAttributeMaxDynamicSharedMemorySize, L_ALLOC);
  k_all<<<256, 512, L_ALLOC, stream>>>((const u16*)d_in[0], (const u16*)d_in[1], (const u16*)d_in[2], (const u16*)d_in[3], (const u16*)d_in[4],
                                     (const u16*)d_in[5], (const u16*)d_in[7], (const u16*)d_in[8], (const u16*)d_in[9], (u16*)d_out, (char*)d_ws);
}
```
